```python
import torch

def _softmax(x, axis=-1):
    return torch.softmax(x, dim=axis)
def _gelu(x):
    return torch.nn.functional.gelu(x, approximate="tanh")
def _silu(x):
    return torch.nn.functional.silu(x)
def _sigmoid(x):
    return torch.sigmoid(x)
def _layernorm(x, axis=-1, eps=1e-5):
    m = x.mean(dim=axis, keepdim=True)
    v = x.var(dim=axis, keepdim=True, unbiased=False)
    return (x - m) / torch.sqrt(v + eps)
def _scatter_add(tgt, idx, src, axis=0):
    return tgt.clone().index_add_(axis, idx, src)
def _segment_sum(src, seg, n):
    out = torch.zeros((n,) + src.shape[1:], dtype=src.dtype)
    return out.index_add_(0, seg, src)
def _pad(x, pw):
    flat = tuple(v for pair in reversed(pw) for v in pair)
    return torch.nn.functional.pad(x, flat)

kernel_name = 'gated_mix__linear_kernel_hedgehog__mixer_s_mlp__prompt'

def setup_inputs():

    _w = (lambda a: (lambda t: t.to(torch.bfloat16) if t.is_floating_point() else t)(torch.as_tensor(a)))
    def _A_setup():
        B, S, D, H = 1, 2048, 448, 16
        Dh = D // H
        return {
            "x": _w((lambda *s: torch.randn(*s, dtype=torch.bfloat16) * 0.05)(B, S, D)),
            "wqkv": _w((lambda *s: torch.randn(*s, dtype=torch.bfloat16) * 1.6)(D, 3 * D)),
            "w_phi": _w((lambda *s: torch.randn(*s, dtype=torch.bfloat16) * 1.6)(H, Dh, Dh) * (Dh ** -0.5)),
            "wo": _w((lambda *s: torch.randn(*s, dtype=torch.bfloat16) * 0.1)(D, D)),
        }
    _A_in = _A_setup()

    def _B_setup():
        B, S, D, F = 1, 2048, 448, 1792
        return {
            "x":  _w((lambda *s: torch.randn(*s, dtype=torch.bfloat16) * 0.05)(B, S, D)),
            "w1": _w((lambda *s: torch.randn(*s, dtype=torch.bfloat16) * 0.4)(D, D) * (D ** -0.5)),
            "w2": _w((lambda *s: torch.randn(*s, dtype=torch.bfloat16) * 0.4)(D, D) * (D ** -0.5)),
            "w3": _w((lambda *s: torch.randn(*s, dtype=torch.bfloat16) * 0.4)(D, F) * (D ** -0.5)),
            "w4": _w((lambda *s: torch.randn(*s, dtype=torch.bfloat16) * 0.4)(F, D) * (F ** -0.5)),
        }
    _B_in = _B_setup()

    return {
        "A_x": _A_in["x"],
        "A_wqkv": _A_in["wqkv"],
        "A_w_phi": _A_in["w_phi"],
        "A_wo": _A_in["wo"],
        "B_x": _B_in["x"],
        "B_w1": _B_in["w1"],
        "B_w2": _B_in["w2"],
        "B_w3": _B_in["w3"],
        "B_w4": _B_in["w4"],
        "gate_w": _w((lambda *s: torch.randn(*s, dtype=torch.bfloat16) * 0.05)(448) * 16.0),
    }

def reference(A_x, A_wqkv, A_w_phi, A_wo, B_x, B_w1, B_w2, B_w3, B_w4, gate_w):
    def _A_ref(x, wqkv, w_phi, wo):
        B, S, D = x.shape
        H = 16; Dh = D // H
        qkv = (lambda x, *d: x.permute(*d))((x @ wqkv).reshape(B, S, 3, H, Dh), 2, 0, 3, 1, 4)
        q, k, v = qkv[0], qkv[1], qkv[2]
        pq = torch.einsum("bhsd,hdf->bhsf", q, w_phi)
        pk = torch.einsum("bhsd,hdf->bhsf", k, w_phi)
        phi_q = torch.concatenate([_softmax(pq, axis=-1), _softmax(-pq, axis=-1)], axis=-1)
        phi_k = torch.concatenate([_softmax(pk, axis=-1), _softmax(-pk, axis=-1)], axis=-1)
        kv = torch.einsum("bhsf,bhsd->bhfd", phi_k, v)
        z  = torch.einsum("bhsf->bhf", phi_k)
        num = torch.einsum("bhqf,bhfd->bhqd", phi_q, kv)
        den = torch.einsum("bhqf,bhf->bhq", phi_q, z)[..., None] + 1e-6
        o = num / den
        return (lambda x, *d: x.permute(*d))(o, 0, 2, 1, 3).reshape(B, S, D) @ wo

    def _B_ref(x, w1, w2, w3, w4):
        B, S, D = x.shape
        h = _gelu(_layernorm(x, axis=-1) @ w1)
        c = D // 4
        g0 = _pad(h[:, :, 0*c:1*c], ((0, 0), (1, 0), (0, 0)))[:, :S, :]
        g1 = _pad(h[:, :, 1*c:2*c], ((0, 0), (0, 1), (0, 0)))[:, 1:, :]
        g2 = _pad(h[:, :, 2*c:3*c], ((0, 0), (2, 0), (0, 0)))[:, :S, :]
        g3 = _pad(h[:, :, 3*c:   ], ((0, 0), (0, 2), (0, 0)))[:, 2:, :]
        hs = torch.concatenate([g0, g1, g2, g3], axis=-1) @ w2
        x1 = x + hs
        h2 = _gelu(_layernorm(x1, axis=-1) @ w3) @ w4
        return x1 + h2

    x = A_x
    g = _sigmoid((x * gate_w).sum(-1, keepdims=True))
    a = _A_ref(x, A_wqkv, A_w_phi, A_wo)
    b = _B_ref(x, B_w1, B_w2, B_w3, B_w4)
    return g * a + (1 - g) * b
```

```cpp
#include <hip/hip_runtime.h>
#include <hip/hip_bf16.h>
typedef short bf16x8 __attribute__((ext_vector_type(8)));
typedef float f32x4 __attribute__((ext_vector_type(4)));
typedef unsigned short u16;
constexpr int S = 2048, D = 448, H = 16, DH = 28, F = 1792;

__device__ __forceinline__ float bf2f(u16 v) { return __uint_as_float((unsigned)v << 16); }
__device__ __forceinline__ u16 f2bf(float f) { __bf16 b = (__bf16)f; return __builtin_bit_cast(u16, b); }
__device__ __forceinline__ float gelu_t(float x) { float u = 0.7978845608028654f * (x + 0.044715f * x * x * x); float e = __expf(2.f * u); float t = 1.f - 2.f / (e + 1.f); return 0.5f * x * (1.f + t); }

__global__ void k_ln(const u16* x, u16* y, const u16* gw, float* gate) {
    int row = blockIdx.x * 4 + (threadIdx.x >> 6), lane = threadIdx.x & 63;
    float v[7]; float s = 0, gsum = 0;
    for (int i = 0; i < 7; ++i) { v[i] = bf2f(x[row * D + lane + 64 * i]); s += v[i]; if (gw) gsum += v[i] * bf2f(gw[lane + 64 * i]); }
    for (int o = 32; o; o >>= 1) { s += __shfl_xor(s, o); gsum += __shfl_xor(gsum, o); }
    float m = s / D, q = 0;
    for (int i = 0; i < 7; ++i) { float d = v[i] - m; q += d * d; }
    for (int o = 32; o; o >>= 1) q += __shfl_xor(q, o);
    float r = rsqrtf(q / D + 1e-5f);
    for (int i = 0; i < 7; ++i) y[row * D + lane + 64 * i] = f2bf((v[i] - m) * r);
    if (gw && lane == 0) gate[row] = 1.f / (1.f + __expf(-gsum));
}

template<int AMODE, int EPI>
__global__ void __launch_bounds__(256) k_gemm(const u16* A, int lda, const u16* W, int ldw, int K, u16* C, int ldc,
                                              const u16* R, const float* gate, const u16* A2, const u16* W2, int K2) {
    __shared__ u16 sA[64][40];
    __shared__ u16 sB[64][40];
    const int tid = threadIdx.x, lane = tid & 63, w = tid >> 6, g = lane >> 4, c = lane & 15;
    const int m0 = blockIdx.y * 64, n0 = blockIdx.x * 64;
    f32x4 acc[4] = {}, acc2[4] = {};
    for (int pass = 0; pass < (EPI == 3 ? 2 : 1); ++pass) {
        const u16* Ap = pass ? A2 : A; const u16* Wp = pass ? W2 : W; int Kp = pass ? K2 : K; int ldap = pass ? D : lda;
        for (int k0 = 0; k0 < Kp; k0 += 32) {
            { int r = tid >> 2, kc = (tid & 3) * 8; int row = m0 + r;
              if (AMODE == 1) { int grp = (k0 + kc) / 112; int sh = grp == 0 ? -1 : grp == 1 ? 1 : grp == 2 ? -2 : 2; row += sh; }
              bf16x8 v = {};
              if (row >= 0 && row < S) v = *(const bf16x8*)(Ap + (size_t)row * ldap + k0 + kc);
              *(bf16x8*)&sA[r][kc] = v; }
            { int kr = tid >> 3, nc = (tid & 7) * 8; bf16x8 v = *(const bf16x8*)(Wp + (size_t)(k0 + kr) * ldw + n0 + nc);
              for (int j = 0; j < 8; ++j) sB[nc + j][kr] = v[j]; }
            __syncthreads();
            bf16x8 a = *(const bf16x8*)&sA[w * 16 + c][g * 8];
            for (int t = 0; t < 4; ++t) { bf16x8 b = *(const bf16x8*)&sB[t * 16 + c][g * 8];
                if (pass == 0) acc[t] = __builtin_amdgcn_mfma_f32_16x16x32_bf16(a, b, acc[t], 0, 0, 0);
                else acc2[t] = __builtin_amdgcn_mfma_f32_16x16x32_bf16(a, b, acc2[t], 0, 0, 0); }
            __syncthreads();
        }
    }
    for (int t = 0; t < 4; ++t) for (int r = 0; r < 4; ++r) {
        int row = m0 + w * 16 + g * 4 + r, col = n0 + t * 16 + c; float v = acc[t][r];
        if (EPI == 1) v = gelu_t(v);
        if (EPI == 2) v += bf2f(R[row * D + col]);
        if (EPI == 3) { float gg = gate[row]; v = gg * acc2[t][r] + (1.f - gg) * (bf2f(R[row * D + col]) + v); }
        C[(size_t)row * ldc + col] = f2bf(v);
    }
}

__device__ __forceinline__ void feat(const u16* qrow, const float* wphi, float* phi) {
    float q[DH]; for (int d = 0; d < DH; ++d) q[d] = bf2f(qrow[d]);
    float p[DH]; float mx = -1e30f, mn = 1e30f;
    for (int f = 0; f < DH; ++f) { float s = 0; for (int d = 0; d < DH; ++d) s += q[d] * wphi[d * DH + f]; p[f] = s; mx = fmaxf(mx, s); mn = fminf(mn, s); }
    float s1 = 0, s2 = 0;
    for (int f = 0; f < DH; ++f) { phi[f] = __expf(p[f] - mx); s1 += phi[f]; phi[DH + f] = __expf(mn - p[f]); s2 += phi[DH + f]; }
    for (int f = 0; f < DH; ++f) { phi[f] /= s1; phi[DH + f] /= s2; }
}
__global__ void __launch_bounds__(128) k_kv(const u16* qkv, const u16* wphi, float* part) {
    __shared__ float sw[DH * DH]; __shared__ float sphi[128][57]; __shared__ float sv[128][29];
    int h = blockIdx.x, ch = blockIdx.y, tid = threadIdx.x;
    for (int i = tid; i < DH * DH; i += 128) sw[i] = bf2f(wphi[h * DH * DH + i]);
    __syncthreads();
    int row = ch * 128 + tid;
    float phi[56];
    feat(qkv + (size_t)row * 3 * D + D + h * DH, sw, phi);
    for (int f = 0; f < 56; ++f) sphi[tid][f] = phi[f];
    for (int d = 0; d < DH; ++d) sv[tid][d] = bf2f(qkv[(size_t)row * 3 * D + 2 * D + h * DH + d]);
    sv[tid][28] = 1.f;
    __syncthreads();
    for (int e = tid; e < 56 * 29; e += 128) { int f = e / 29, d = e % 29; float s = 0; for (int r = 0; r < 128; ++r) s += sphi[r][f] * sv[r][d]; part[((size_t)(ch * H + h) * 56 + f) * 29 + d] = s; }
}
__global__ void __launch_bounds__(128) k_o(const u16* qkv, const u16* wphi, const float* part, u16* o) {
    __shared__ float sw[DH * DH]; __shared__ float skv[56 * 29];
    int h = blockIdx.x, ch = blockIdx.y, tid = threadIdx.x;
    for (int i = tid; i < DH * DH; i += 128) sw[i] = bf2f(wphi[h * DH * DH + i]);
    for (int e = tid; e < 56 * 29; e += 128) { float s = 0; for (int c2 = 0; c2 < 16; ++c2) s += part[(size_t)(c2 * H + h) * 56 * 29 + e]; skv[e] = s; }
    __syncthreads();
    int row = ch * 128 + tid;
    float phi[56];
    feat(qkv + (size_t)row * 3 * D + h * DH, sw, phi);
    float den = 1e-6f; for (int f = 0; f < 56; ++f) den += phi[f] * skv[f * 29 + 28];
    for (int d = 0; d < DH; ++d) { float s = 0; for (int f = 0; f < 56; ++f) s += phi[f] * skv[f * 29 + d]; o[(size_t)row * D + h * DH + d] = f2bf(s / den); }
}

extern "C" void kernel_launch(void* const* d_in, const int* in_sizes, int n_in, void* d_out, int out_size, void* d_ws, size_t ws_size, hipStream_t stream) {
    const u16* x = (const u16*)d_in[0]; const u16* wqkv = (const u16*)d_in[1]; const u16* wphi = (const u16*)d_in[2]; const u16* wo = (const u16*)d_in[3];
    const u16* w1 = (const u16*)d_in[5]; const u16* w2 = (const u16*)d_in[6]; const u16* w3 = (const u16*)d_in[7]; const u16* w4 = (const u16*)d_in[8]; const u16* gw = (const u16*)d_in[9];
    char* ws = (char*)d_ws;
    u16* xn = (u16*)ws; ws += S * D * 2;
    u16* qkv = (u16*)ws; ws += S * 3 * D * 2;
    u16* h = (u16*)ws; ws += S * D * 2;
    u16* x1 = (u16*)ws; ws += S * D * 2;
    u16* x1n = (u16*)ws; ws += S * D * 2;
    u16* h3 = (u16*)ws; ws += S * F * 2;
    u16* o = (u16*)ws; ws += S * D * 2;
    float* gate = (float*)ws; ws += S * 4;
    float* part = (float*)ws;
    u16* out = (u16*)d_out;
    k_ln<<<S / 4, 256, 0, stream>>>(x, xn, gw, gate);
    k_gemm<0, 0><<<dim3(21, 32), 256, 0, stream>>>(x, D, wqkv, 3 * D, D, qkv, 3 * D, nullptr, nullptr, nullptr, nullptr, 0);
    k_gemm<0, 1><<<dim3(7, 32), 256, 0, stream>>>(xn, D, w1, D, D, h, D, nullptr, nullptr, nullptr, nullptr, 0);
    k_kv<<<dim3(16, 16), 128, 0, stream>>>(qkv, wphi, part);
    k_o<<<dim3(16, 16), 128, 0, stream>>>(qkv, wphi, part, o);
    k_gemm<1, 2><<<dim3(7, 32), 256, 0, stream>>>(h, D, w2, D, D, x1, D, x, nullptr, nullptr, nullptr, 0);
    k_ln<<<S / 4, 256, 0, stream>>>(x1, x1n, nullptr, nullptr);
    k_gemm<0, 1><<<dim3(28, 32), 256, 0, stream>>>(x1n, D, w3, F, D, h3, F, nullptr, nullptr, nullptr, nullptr, 0);
    k_gemm<0, 3><<<dim3(7, 32), 256, 0, stream>>>(h3, F, w4, D, F, out, D, x1, gate, o, wo, D);
}
```

```cpp
#include <hip/hip_runtime.h>
#include <hip/hip_bf16.h>

namespace {
typedef unsigned short u16;
typedef unsigned u32;
typedef unsigned long long u64;
typedef short bf16x8 __attribute__((ext_vector_type(8)));
typedef short s16x4 __attribute__((ext_vector_type(4)));
typedef float f32x4 __attribute__((ext_vector_type(4)));
typedef float f32x2 __attribute__((ext_vector_type(2)));
typedef __bf16 bf16x2 __attribute__((ext_vector_type(2)));
typedef u32 u32x4 __attribute__((ext_vector_type(4)));
typedef u32 u32x2 __attribute__((ext_vector_type(2)));
#define LAS __attribute__((address_space(3)))
#define GAS __attribute__((address_space(1)))
#define DI __device__ __forceinline__
typedef __amdgpu_buffer_rsrc_t rsrc_t;

constexpr int SEQ = 2048, DM = 448, FF = 1792;

constexpr u32 E0 = 0, E1 = 28672, E2 = 57344, E3 = 86016, E4 = 114688, XX = 143360, MR = 151552, MISC = 159744;
constexpr u32 LDS_BYTES = 163840;
constexpr u32 L_SUM = MISC, L_SQ = MISC + 256, L_GATE = MISC + 512, L_SUM1 = MISC + 768, L_SQ1 = MISC + 1024, L_CS1 = MISC + 1280, GWL = MISC + 2048;
constexpr u32 WPHI = MR;
constexpr u32 XT_CB = 8192;

constexpr u32 WS_EPOCH = 0;
constexpr u32 WS_FCLR = 4096;
constexpr u32 WS_FKV = 12288;
constexpr u32 WS_FRS = 16384;
constexpr u32 WS_H = 65536;
constexpr u32 WS_X1 = WS_H + 32 * 57344;
constexpr u32 WS_KVP = WS_X1 + 32 * 57344;
constexpr u32 WS_KVF = WS_KVP + 16 * 32 * 8192;
constexpr u32 WS_RS = WS_KVF + 16 * 4096;
constexpr u32 WS_END = WS_RS + 32 * 64 * 7168;
constexpr u32 SPIN_CAP = 1u << 16;
constexpr int SC1 = 16;
#define CODE_LINES 0

DI f32x4 mfma(u32x4 a, u32x4 b, f32x4 c) {
    return __builtin_amdgcn_mfma_f32_16x16x32_bf16(__builtin_bit_cast(bf16x8, a), __builtin_bit_cast(bf16x8, b), c, 0, 0, 0);
}
DI u32 pk(float a, float b) { f32x2 f = {a, b}; return __builtin_bit_cast(u32, __builtin_convertvector(f, bf16x2)); }
DI u32 pk2(f32x2 f) { return __builtin_bit_cast(u32, __builtin_convertvector(f, bf16x2)); }
DI u32x4 pk8(f32x4 a, f32x4 b) { return u32x4{pk(a[0], a[1]), pk(a[2], a[3]), pk(b[0], b[1]), pk(b[2], b[3])}; }
DI float blo(u32 v) { return __builtin_bit_cast(float, v << 16); }
DI float bhi(u32 v) { return __builtin_bit_cast(float, v & 0xffff0000u); }
DI u32x4 lds128(u32 a) { return *(LAS const u32x4*)a; }
DI u32x2 lds64(u32 a) { return *(LAS const u32x2*)a; }
DI float ldsf(u32 a) { return *(LAS const float*)a; }
DI void sts128(u32 a, u32x4 v) { *(LAS u32x4*)a = v; }
DI void stsf(u32 a, float v) { *(LAS float*)a = v; }
DI u32x4 ldw2(u32 a0, u32 a1) {
    s16x4 lo = __builtin_amdgcn_ds_read_tr16_b64_v4i16((LAS s16x4*)a0);
    s16x4 hi = __builtin_amdgcn_ds_read_tr16_b64_v4i16((LAS s16x4*)a1);
    u32x2 l = __builtin_bit_cast(u32x2, lo), h = __builtin_bit_cast(u32x2, hi);
    return u32x4{l.x, l.y, h.x, h.y};
}
DI u32x4 ldw(u32 a) { return ldw2(a, a + 512); }
DI void dma16(rsrc_t rs, u32 voff, u32 soff, u32 lds) {
    asm volatile("s_mov_b32 m0, %0\n\ts_nop 0\n\tbuffer_load_dwordx4 %1, %2, %3 offen lds" ::"s"(lds), "v"(voff), "s"(rs), "s"(soff) : "memory");
}
DI void dma_pair(rsrc_t rs, u32 v0, u32 v1, u32 soff, u32 lds) {
    asm volatile("s_mov_b32 m0, %0\n\ts_nop 0\n\t"
                 "buffer_load_dwordx4 %1, %3, %4 offen lds\n\t"
                 "buffer_load_dwordx4 %2, %3, %4 offen offset:1024 lds"
                 ::"s"(lds), "v"(v0), "v"(v1), "s"(rs), "s"(soff) : "memory");
}
#define WAITV(n) asm volatile("s_waitcnt vmcnt(" #n ")" ::: "memory")
#define SLEEP(n) asm volatile("s_sleep " #n ::: "memory")
#define BAR() asm volatile("s_waitcnt lgkmcnt(0)\n\ts_barrier" ::: "memory")
#define SB() __builtin_amdgcn_sched_barrier(0)
DI u32x4 ldg128(rsrc_t rs, u32 off) { return __builtin_amdgcn_raw_buffer_load_b128(rs, off, 0, SC1); }
DI u32x2 ldg64(rsrc_t rs, u32 off) { return __builtin_amdgcn_raw_buffer_load_b64(rs, off, 0, SC1); }
DI void stg128(rsrc_t rs, u32 off, u32x4 v) { __builtin_amdgcn_raw_buffer_store_b128(v, rs, off, 0, SC1); }
DI void stg64(rsrc_t rs, u32 off, u32x2 v) { __builtin_amdgcn_raw_buffer_store_b64(v, rs, off, 0, SC1); }
DI u32 san(u32 v) { return v < 0xfffffffeu ? v : 0xfffffffeu; }
DI u32 umax3(u32 a, u32 b, u32 c) { u32 m = a > b ? a : b; return m > c ? m : c; }
DI u32 fmax4(u32 m, u32x4 v) { return umax3(umax3(m, v.x, v.y), v.z, v.w); }
template <int N> DI bool has_empty(const u32x4 (&f)[N]) {
    u32 m = 0;
#pragma unroll
    for (int i = 0; i < N; ++i) m = fmax4(m, f[i]);
    return __any(m == 0xffffffffu);
}
DI f32x2 gelu2(f32x2 y, float k1, float k2, float so) {
    f32x2 t = __builtin_elementwise_fma(y * y, f32x2{k2, k2}, f32x2{k1, k1}) * y;
    f32x2 d = f32x2{__builtin_amdgcn_exp2f(t.x), __builtin_amdgcn_exp2f(t.y)} + 1.f;
    f32x2 r = {__builtin_amdgcn_rcpf(d.x), __builtin_amdgcn_rcpf(d.y)};
    return (y * so) * r;
}
constexpr float GC1 = 2.3022082f, GC2 = 0.10294324f;
DI float f_of(u32 v) { return __builtin_bit_cast(float, v); }
DI u32 u_of(float v) { return __builtin_bit_cast(u32, v); }
DI float xg_sum(float v) {
    auto r = __builtin_amdgcn_permlane32_swap(u_of(v), u_of(v), false, false);
    v = f_of(r[0]) + f_of(r[1]);
    auto s = __builtin_amdgcn_permlane16_swap(u_of(v), u_of(v), false, false);
    return f_of(s[0]) + f_of(s[1]);
}
DI float xg_max(float v) {
    auto r = __builtin_amdgcn_permlane32_swap(u_of(v), u_of(v), false, false);
    v = fmaxf(f_of(r[0]), f_of(r[1]));
    auto s = __builtin_amdgcn_permlane16_swap(u_of(v), u_of(v), false, false);
    return fmaxf(f_of(s[0]), f_of(s[1]));
}
template <int CTRL> DI float dpp(float v) {
    return __builtin_bit_cast(float, __builtin_amdgcn_update_dpp(0, __builtin_bit_cast(int, v), CTRL, 0xf, 0xf, true));
}
DI float row_sum(float v) { v += dpp<0x128>(v); v += dpp<0x124>(v); v += dpp<0x122>(v); v += dpp<0x121>(v); return v; }
DI float row_max(float v) {
    asm volatile("s_nop 1\n\tv_max_f32_dpp %0, %0, %0 row_ror:8 row_mask:0xf bank_mask:0xf\n\ts_nop 1\n\t"
                 "v_max_f32_dpp %0, %0, %0 row_ror:4 row_mask:0xf bank_mask:0xf\n\ts_nop 1\n\t"
                 "v_max_f32_dpp %0, %0, %0 row_ror:2 row_mask:0xf bank_mask:0xf\n\ts_nop 1\n\t"
                 "v_max_f32_dpp %0, %0, %0 row_ror:1 row_mask:0xf bank_mask:0xf" : "+v"(v));
    return v;
}
DI u32x4 swap_units(u32 a0, u32 a1, u32 b0, u32 b1) {
    auto r0 = __builtin_amdgcn_permlane16_swap(a0, b0, false, false);
    auto r1 = __builtin_amdgcn_permlane16_swap(a1, b1, false, false);
    return u32x4{(u32)r0[0], (u32)r1[0], (u32)r0[1], (u32)r1[1]};
}
constexpr float LOG2E = 1.4426950408889634f;
DI float diag4(f32x4 d, u32 c) { float a = (c & 1) ? d[1] : d[0], b = (c & 1) ? d[3] : d[2]; return (c & 2) ? b : a; }

#define PTA(id)
#define PTB(id)

#define PIN(x) asm volatile("" : "+v"(x))
#define SYNC(id) do { PTA(id); BAR(); } while (0)
#define SYNCW(n, id) do { PTA(id); WAITV(n); PTB(id); BAR(); } while (0)
constexpr int NWAVE = 11;

template <int K0>
DI void proj7(u32 wa0, u32 wa1, const u32x4 (&xf)[14], f32x4 (&acc)[2]) {
    u32x4 f[4][2];
    PIN(wa0); PIN(wa1);
#pragma unroll
    for (int k = 0; k < 3; ++k) { f[k][0] = ldw(wa0 + k * 4096); f[k][1] = ldw(wa1 + k * 4096); }
    SB();
#pragma unroll
    for (int k = 0; k < 7; ++k) {
        if (k + 3 < 7) { f[(k + 3) & 3][0] = ldw(wa0 + (k + 3) * 4096); f[(k + 3) & 3][1] = ldw(wa1 + (k + 3) * 4096); }
        acc[0] = mfma(f[k & 3][0], xf[K0 + k], acc[0]);
        acc[1] = mfma(f[k & 3][1], xf[K0 + k], acc[1]);
        PIN(acc[0]); PIN(acc[1]);
        SB();
    }
}

DI void features(const f32x4 (&a)[2], const u32x4 (&wpf)[2], u32 g, u32x4& pos, u32x4& neg) {
    u32x4 qf = pk8(a[0], a[1]);
    if (g == 3) { qf.z = 0; qf.w = 0; }
    f32x4 p0 = mfma(wpf[0], qf, f32x4{}) * LOG2E, p1 = mfma(wpf[1], qf, f32x4{}) * LOG2E;
    const bool v1 = g < 3;
    if (!v1) p1 = p0;
    float mx = fmaxf(fmaxf(fmaxf(p0[0], p0[1]), fmaxf(p0[2], p0[3])), fmaxf(fmaxf(p1[0], p1[1]), fmaxf(p1[2], p1[3])));
    float mn = fminf(fminf(fminf(p0[0], p0[1]), fminf(p0[2], p0[3])), fminf(fminf(p1[0], p1[1]), fminf(p1[2], p1[3])));
    mx = xg_max(mx); mn = -xg_max(-mn);
    const f32x4 d0 = p0 - mx, d1 = p1 - mx, e0 = mn - p0, e1 = mn - p1;
    f32x4 a0, a1, b0, b1;
#pragma unroll
    for (int r = 0; r < 4; ++r) {
        a0[r] = __builtin_amdgcn_exp2f(d0[r]); a1[r] = __builtin_amdgcn_exp2f(d1[r]);
        b0[r] = __builtin_amdgcn_exp2f(e0[r]); b1[r] = __builtin_amdgcn_exp2f(e1[r]);
    }
    if (!v1) { a1 = f32x4{}; b1 = f32x4{}; }
    const f32x4 sa4 = a0 + a1, sb4 = b0 + b1;
    const float ra = __builtin_amdgcn_rcpf(xg_sum((sa4[0] + sa4[1]) + (sa4[2] + sa4[3])));
    const float rb = __builtin_amdgcn_rcpf(xg_sum((sb4[0] + sb4[1]) + (sb4[2] + sb4[3])));
    pos = pk8(a0 * ra, a1 * ra);
    neg = pk8(b0 * rb, b1 * rb);
}

__global__ void __launch_bounds__(NWAVE * 64, 3)
fused_kernel(const u16* __restrict__ x, const u16* __restrict__ wqkv, const u16* __restrict__ wphi, const u16* __restrict__ wo,
             const u16* __restrict__ w1, const u16* __restrict__ w2, const u16* __restrict__ w3, const u16* __restrict__ w4,
             const u16* __restrict__ gw, u16* out, unsigned char* ws) {
    extern __shared__ __attribute__((aligned(16))) unsigned char smem[];
    const u32 tid = threadIdx.x, lane = tid & 63;
    const u32 w = __builtin_amdgcn_readfirstlane(tid >> 6);
    const u32 g = lane >> 4, c = lane & 15;
    const u32 b = blockIdx.x;
    const u32 rt = 8 * ((b & 7) >> 1) + (b >> 5), j = 4 * (b & 1) + ((b >> 3) & 3);
    const int row0 = rt * 64;
    PTA(0);
    const rsrc_t rWS = __builtin_amdgcn_make_buffer_rsrc((void*)ws, 0, WS_END, 0x00020000);
    const bool hasB = j < 7;
    const u32 psnS = hasB ? 0u : 0x40000000u;
    const u32 odd = j & 1;
    const u32 q4 = (lane & 15) >> 2, pp = lane & 3;
    const u32 trs = ((q4 >> 1) & 1) | ((g & 1) << 1);
    const u32 trb = (8 * g + q4) * 128 + (pp >> 1) * 16 + (pp & 1) * 8;
#define TRA(t) (trb + (((t) ^ trs) << 5))
    const u32x4 ONES = {0x3f803f80u, 0x3f803f80u, 0x3f803f80u, 0x3f803f80u};
    const u32x4 EMPTY = {0xffffffffu, 0xffffffffu, 0xffffffffu, 0xffffffffu};

    if (w == 8 || w == 9) {
        const rsrc_t rX = __builtin_amdgcn_make_buffer_rsrc((void*)x, 0, SEQ * DM * 2, 0x00020000);
        const rsrc_t rQKV = __builtin_amdgcn_make_buffer_rsrc((void*)wqkv, 0, DM * 3 * DM * 2, 0x00020000);
        const rsrc_t rWO = __builtin_amdgcn_make_buffer_rsrc((void*)wo, 0, DM * DM * 2, 0x00020000);
        const rsrc_t rW1 = __builtin_amdgcn_make_buffer_rsrc((void*)w1, 0, DM * DM * 2, 0x00020000);
        const rsrc_t rW2 = __builtin_amdgcn_make_buffer_rsrc((void*)w2, 0, DM * DM * 2, 0x00020000);
        const rsrc_t rW3 = __builtin_amdgcn_make_buffer_rsrc((void*)w3, 0, DM * FF * 2, 0x00020000);
        const rsrc_t rW4 = __builtin_amdgcn_make_buffer_rsrc((void*)w4, 0, DM * FF * 2, 0x00020000);
        const u32 lh = w - 8, lo = lh * 2048;
        const u32 r8 = lane >> 3, sl = lane & 7;
        const u32 chA = sl ^ (((r8 >> 1) & 1) << 1), chB = chA ^ 4;
        const u32 jb = hasB ? j : 0;
        {
            const u32 xa = (row0 + 16 * lh + r8) * 896 + ((sl ^ (r8 >> 1)) * 16), xb = (xa ^ 64) + 8 * 896 - 1024;
#pragma unroll 1
            for (u32 u = 0; u < 7; ++u) {
                dma_pair(rX, xa, xb, u * 128, u * XT_CB + lo);
                dma_pair(rX, xa, xb, u * 128 + 32 * 896, u * XT_CB + 4096 + lo);
            }
        }
        const u32 sA = (16 * lh + r8) * 896 + chA * 16, sB = (16 * lh + 8 + r8) * 896 + chB * 16 - 1024;
#define LD_STD(rs, half, ent, u0, u1) do { _Pragma("unroll 1") for (u32 u = (u0); u < (u1); ++u) dma_pair(rs, sA, sB, (32 * (7 * (half) + u)) * 896 + jb * 128, (ent) + u * 4096 + lo); } while (0)
        const u32 qA = (16 * lh + r8) * 2688 + (chA >> 2) * 56 + (chA & 3) * 16, qB = (16 * lh + 8 + r8) * 2688 + (chB >> 2) * 56 + (chB & 3) * 16 - 1024;
#define LD_QKV(sec, half, ent, u0, u1) do { _Pragma("unroll 1") for (u32 u = (u0); u < (u1); ++u) dma_pair(rQKV, qA, qB, (32 * (7 * (half) + u)) * 2688 + (sec) * 896 + 112 * j, (ent) + u * 4096 + lo); } while (0)
        const u32 tA = (16 * lh + r8) * 3584 + chA * 16, tB = (16 * lh + 8 + r8) * 3584 + chB * 16 - 1024;
        const u32 uA = (16 * lh + r8 + (chA >> 2) * 32) * 3584 + (chA & 3) * 16, uB = (16 * lh + 8 + r8 + (chB >> 2) * 32) * 3584 + (chB & 3) * 16 - 1024;
        const u32 s3f = 448 * j + odd * 64, s3h = 448 * j + (odd ? 0 : 384);
#define LD_W3(i, ent) do { _Pragma("unroll") for (u32 u = 0; u < 6; ++u) dma_pair(rW3, tA, tB, (64 * (i) + 32 * (u / 3)) * 3584 + 128 * (u % 3) + s3f, (ent) + u * 4096 + lo); \
                           dma_pair(rW3, uA, uB, (64 * (i)) * 3584 + s3h, (ent) + 6 * 4096 + lo); } while (0)
        const u32 pr = 16 * (r8 >> 2) + (r8 & 3) + 8 * lh;
        const u32 pA = pr * 896 + chA * 16, pB = (pr + 4) * 896 + chB * 16 - 1024;
        const u32 s4f = (224 * j + odd * 32) * 896, s4l = (224 * j + (odd ? 0 : 192)) * 896;
#define LD_W4(cb, ent) do { _Pragma("unroll 1") for (u32 u = 0; u < 6; ++u) dma_pair(rW4, pA, pB, s4f + u * 32 * 896 + (cb) * 128, (ent) + u * 4096 + lo); \
                            dma_pair(rW4, pA, pB, s4l + (cb) * 128, (ent) + 6 * 4096 + lo); } while (0)
#define LD_WO(half, ent) do { _Pragma("unroll 1") for (u32 u = 0; u < 7; ++u) { const u32 uu = 7 * (half) + u; dma_pair(rWO, pA, pB, (56 * j + 28 * (uu & 1)) * 896 + (uu >> 1) * 128, (ent) + u * 4096 + lo); } } while (0)
        LD_STD(rW1, 0, E2, 0, 4);
        SYNCW(8, 1);
        LD_STD(rW1, 0, E2, 4, 7);
        LD_STD(rW1, 1, E3, 0, 4);
        SYNCW(8, 2);
        LD_STD(rW1, 1, E3, 4, 7);
        LD_QKV(1, 0, E4, 0, 3);
        SYNCW(6, 3);
        LD_QKV(1, 0, E4, 3, 7);
        SYNCW(0, 4);
        LD_QKV(2, 0, E0, 0, 7);
        LD_STD(rW2, 0, E1, 0, 4);
        SYNCW(8, 5);
        LD_STD(rW2, 0, E1, 4, 7);
        LD_STD(rW2, 1, E2, 0, 7);
        LD_QKV(1, 1, E3, 0, 3);
        SYNCW(6, 6);
        LD_QKV(1, 1, E3, 3, 7);
        LD_QKV(2, 1, E4, 0, 7);
        LD_QKV(0, 0, E0, 0, 7);
        SYNCW(28, 7);
        LD_QKV(0, 1, E1, 0, 7);
        SYNCW(28, 8);
        LD_W3(0, E3);
        SYNCW(14, 9);
        LD_W3(1, E4);
        SYNC(10);
        SYNCW(14, 12);
#pragma unroll 1
        for (u32 i = 0; i < 5; ++i) {
            const u32 e = (i % 3 == 0) ? E2 : ((i % 3 == 1) ? E3 : E4);
            LD_W3(i + 2, e);
            SYNCW(14, 13 + i);
        }
        LD_W4(0, E4);
        SYNCW(14, 18);
        LD_W4(1, E2);
        SYNC(19);
        LD_WO(0, E0);
        LD_WO(1, E3);
        SYNC(20);
        SYNCW(0, 21);
#pragma unroll 1
        for (u32 u0 = 0; u0 < 5; ++u0) {
            const u32 e = (u0 % 3 == 0) ? E1 : ((u0 % 3 == 1) ? E4 : E2);
            LD_W4(u0 + 2, e);
            SYNCW(14, 22 + u0);
        }
        SYNCW(0, 27);
        SYNC(28);
        SYNC(29);
    } else if (w == 10) {
        const rsrc_t rPHI = __builtin_amdgcn_make_buffer_rsrc((void*)wphi, 0, 16 * 28 * 28 * 2, 0x00020000);
        const rsrc_t rGW = __builtin_amdgcn_make_buffer_rsrc((void*)gw, 0, DM * 2, 0x00020000);
        const u32 head = 2 * j + (rt >> 4), chunk = rt & 15;
        dma16(rGW, lane * 16, 0, GWL);
        dma_pair(rPHI, lane * 16, lane * 16, j * 3136, WPHI);
        dma_pair(rPHI, lane * 16, lane * 16, j * 3136 + 2048, WPHI + 2048);
        const u32 tag = __builtin_amdgcn_readfirstlane(__builtin_amdgcn_raw_buffer_load_b32(rWS, WS_EPOCH, 0, SC1)) + 1;
        const u32 taghi = tag * 0x9E3779B1u ^ 0x5bd1e995u;
        if (lane < 16) stg128(rWS, WS_KVF + head * 4096 + chunk * 256 + lane * 16, EMPTY);
        SYNC(1);
        WAITV(0);
        SYNC(2);
        if (lane == 0) stg64(rWS, WS_FCLR + rt * 128 + j * 8, u32x2{tag, taghi});
        f32x4 cs[4] = {};
        u32 ca[4];
#pragma unroll
        for (int t = 0; t < 4; ++t) { ca[t] = E2 + TRA(t); PIN(ca[t]); }
#pragma unroll
        for (int hlf = 0; hlf < 2; ++hlf) {
            u32x4 f[2][4];
#pragma unroll
            for (int t = 0; t < 4; ++t) f[0][t] = ldw(ca[t] + hlf * (E3 - E2));
#pragma unroll
            for (int k = 0; k < 7; ++k) {
                if (k < 6) {
#pragma unroll
                    for (int t = 0; t < 4; ++t) f[(k + 1) & 1][t] = ldw(ca[t] + hlf * (E3 - E2) + (k + 1) * 4096);
                }
                SB();
#pragma unroll
                for (int t = 0; t < 4; ++t) { cs[t] = mfma(f[k & 1][t], ONES, cs[t]); PIN(cs[t]); }
                SB();
            }
            if (hlf == 0) SYNC(3);
        }
        if (c == 0) {
#pragma unroll
            for (int t = 0; t < 4; ++t) sts128(L_CS1 + (16 * t + 4 * g) * 4, __builtin_bit_cast(u32x4, cs[t]));
        }
        SYNC(4);
        {
            const u32 l24 = lane < 24 ? lane : 0;
            int nb = (int)rt - 1 + (int)(l24 >> 3);
            nb = nb < 0 ? 0 : (nb > 31 ? 31 : nb);
            const u32 fo = WS_FCLR + nb * 128 + (l24 & 7) * 8;
            _Pragma("unroll 1") for (u32 sp = 0; sp < SPIN_CAP; ++sp) {
                u32x2 fl = ldg64(rWS, fo);
                if (__all(fl.x == tag && fl.y == taghi)) break;
                SLEEP(2);
            }
        }
#pragma unroll 1
        for (int i = 5; i <= 10; ++i) SYNC(i);
        if (lane == 0) stg64(rWS, WS_FKV + j * 256 + rt * 8, u32x2{tag, taghi});
        int state = 0;
        const u32 fo = WS_FKV + j * 256 + (lane & 31) * 8;
        const u32 poff = WS_KVP + (head * 32 + g) * 4096 + chunk * 256 + c * 16;
        u32x4 pv[8];
        pv[0] = u32x4{0, 0, 0, 0};
#pragma unroll 1
        for (u32 it = 12; it < 20 + SPIN_CAP; ++it) {
            if (state == 0) {
                u32x2 fl = ldg64(rWS, fo); pv[0].x = fl.x; pv[0].y = fl.y; state = 1;
            } else if (state == 1) {
                if (__all(pv[0].x == tag && pv[0].y == taghi)) {
#pragma unroll
                    for (int i = 0; i < 8; ++i) pv[i] = ldg128(rWS, poff + i * 16384);
                    state = 2;
                } else { u32x2 fl = ldg64(rWS, fo); pv[0].x = fl.x; pv[0].y = fl.y; }
            } else if (state == 2) {
                u32x4 o;
#pragma unroll
                for (int e = 0; e < 4; ++e) {
                    f32x2 s = {0.f, 0.f};
#pragma unroll
                    for (int i = 0; i < 8; ++i) s += f32x2{blo(pv[i][e]), bhi(pv[i][e])};
                    o[e] = san(pk(xg_sum(s.x), xg_sum(s.y)));
                }
                if (lane < 16) stg128(rWS, WS_KVF + head * 4096 + chunk * 256 + lane * 16, o);
                state = 3;
            } else if (state == 3 || (state == 4 && has_empty(pv))) {
#pragma unroll
                for (int i = 0; i < 8; ++i) pv[i] = ldg128(rWS, WS_KVF + 2 * j * 4096 + lane * 16 + i * 1024);
                state = 4;
            } else if (state == 4) {
#pragma unroll
                for (int i = 0; i < 8; ++i) sts128(MR + i * 1024 + lane * 16, pv[i]);
                state = 5;
            }
            if (it < 20) { SYNC(it); }
            else { if (state == 5) break; asm volatile("" ::: "memory"); }
        }
#pragma unroll 1
        for (int i = 20; i <= 28; ++i) SYNC(i);
        if (lane == 0) stg64(rWS, WS_FRS + rt * 128 + j * 8, u32x2{tag, taghi});
        _Pragma("unroll 1") for (u32 sp = 0; sp < SPIN_CAP; ++sp) {
            u32x2 fl = ldg64(rWS, WS_FRS + rt * 128 + (lane & 7) * 8);
            if (__all(fl.x == tag && fl.y == taghi)) break;
            asm volatile("" ::: "memory");
        }
        if (b == 0 && lane == 0) __builtin_amdgcn_raw_buffer_store_b32(tag, rWS, WS_EPOCH, 0, SC1);
        SYNC(29);
    } else {
        const u32 mt = w & 3, hf = w >> 2;
        const u32 myrow = 16 * mt + c;
        u32 hoff, x1o;
        {
            const u32 nt = 2 * hf + (g & 1);
            const u32 grp = (4 * j + nt) / 7;
            const int dlt = grp == 0 ? -1 : (grp == 1 ? 1 : (grp == 2 ? -2 : 2));
            const u32 uu = 2 * nt + (g >> 1);
            const u32 cpart = (((2 * j + (uu >> 2)) * 64 + (uu & 3) * 16) * 16) | psnS;
            const int R = row0 + myrow, Rd = R - dlt;
            hoff = (u32)Rd < (u32)SEQ ? WS_H + cpart + (Rd >> 4) * (14 * 1024) + (Rd & 15) * 16 : 0xfffffff0u;
            const u32 own = cpart + (R >> 4) * (14 * 1024) + (R & 15) * 16;
            x1o = WS_X1 + own;
            stg128(rWS, hoff, EMPTY);
            stg128(rWS, x1o, EMPTY);
            if (rt == 0 || rt == 31)
                stg128(rWS, (u32)(R + dlt) < (u32)SEQ ? 0xfffffff0u : WS_H + own, u32x4{0, 0, 0, 0});
        }
        SYNC(1);
        u32x4 xf[14];
        u32x2 xres[2];
        u32x4 wpf[2];
        {
            const u32 sx = (myrow >> 1) & 7;
            const u32 a0 = myrow * 128 + ((g ^ sx) * 16), a1 = a0 ^ 64;
#pragma unroll
            for (int ks = 0; ks < 14; ++ks) xf[ks] = lds128((ks >> 1) * XT_CB + ((ks & 1) ? a1 : a0));
#pragma unroll
            for (int t = 0; t < 2; ++t) xres[t] = lds64((hasB ? j : 0) * XT_CB + myrow * 128 + (((2 * (2 * hf + t) + (g >> 1)) ^ sx) * 16) + (g & 1) * 8);
#pragma unroll
            for (int ft = 0; ft < 2; ++ft) {
                u32 a = WPHI + hf * 1568 + (4 * g + q4) * 56 + ft * 32 + pp * 8;
                u32x4 t = ldw2(a, a + 16 * 56);
                if (g == 3) { t.z = 0; t.w = 0; }
                wpf[ft] = t;
            }
        }
        {
            f32x4 s0 = {}, s1 = {};
            if (hf == 0) {
#pragma unroll
                for (int ks = 0; ks < 14; ++ks) s0 = mfma(lds128(GWL + ks * 64 + g * 16), xf[ks], s0);
                if (g == 0) stsf(L_GATE + myrow * 4, __builtin_amdgcn_rcpf(1.f + __builtin_amdgcn_exp2f(-LOG2E * s0[0])));
            } else {
#pragma unroll
                for (int ks = 0; ks < 14; ++ks) { s0 = mfma(ONES, xf[ks], s0); s1 = mfma(xf[ks], xf[ks], s1); }
                if (g == (c >> 2)) { stsf(L_SUM + myrow * 4, s0[0]); stsf(L_SQ + myrow * 4, diag4(s1, c)); }
            }
        }
        const u32 ta0 = TRA(2 * hf), ta1 = TRA(2 * hf + 1);
        {
            f32x4 a1c[2] = {};
            WAITV(0);
            SYNC(2);
            proj7<0>(E2 + ta0, E2 + ta1, xf, a1c);
            SYNC(3);
            proj7<7>(E3 + ta0, E3 + ta1, xf, a1c);
            SYNC(4);
            const float mean = ldsf(L_SUM + myrow * 4) * (1.f / DM);
            const float var = fmaxf(ldsf(L_SQ + myrow * 4) * (1.f / DM) - mean * mean, 0.f);
            const float rstd = __builtin_amdgcn_rsqf(var + 1e-5f);
            const float k1 = -GC1 * rstd, k2 = -GC2 * rstd * rstd * rstd;
            u32 pkd[2][2];
#pragma unroll
            for (int t = 0; t < 2; ++t) {
                const f32x4 cs = __builtin_bit_cast(f32x4, lds128(L_CS1 + (16 * (2 * hf + t) + 4 * g) * 4));
                f32x2 y0 = f32x2{a1c[t][0], a1c[t][1]} - mean * f32x2{cs[0], cs[1]}, y1 = f32x2{a1c[t][2], a1c[t][3]} - mean * f32x2{cs[2], cs[3]};
                pkd[t][0] = san(pk2(gelu2(y0, k1, k2, rstd))); pkd[t][1] = san(pk2(gelu2(y1, k1, k2, rstd)));
            }
            stg128(rWS, hoff, swap_units(pkd[0][0], pkd[0][1], pkd[1][0], pkd[1][1]));
        }
        f32x4 ak[2] = {}, av[2] = {};
        proj7<0>(E4 + ta0, E4 + ta1, xf, ak);
        SYNC(5);
        proj7<0>(E0 + ta0, E0 + ta1, xf, av);
        {
            f32x4 a2[4] = {};
            u32x4 hfr[7];
            const u32 goff = (WS_H + rt * 57344 + ((mt * 14 + 7 * hf) * 64 + lane) * 16) | psnS;
#pragma unroll
            for (int i = 0; i < 7; ++i) hfr[i] = ldg128(rWS, goff + i * 1024);
            SYNC(6);
            _Pragma("unroll 1") for (u32 sp = 0; sp < SPIN_CAP && has_empty(hfr); ++sp) {
                SLEEP(4);
#pragma unroll
                for (int i = 0; i < 7; ++i) hfr[i] = ldg128(rWS, goff + i * 1024);
            }
            PTB(6);
            const u32 wb = hf ? E2 : E1;
            u32 t0 = wb + TRA(0), t1 = wb + TRA(1), t2 = wb + TRA(2), t3 = wb + TRA(3);
            PIN(t0); PIN(t1); PIN(t2); PIN(t3);
            u32x4 f[2][4];
            f[0][0] = ldw(t0); f[0][1] = ldw(t1); f[0][2] = ldw(t2); f[0][3] = ldw(t3);
            SB();
#pragma unroll
            for (int i = 0; i < 7; ++i) {
                if (i < 6) { f[(i + 1) & 1][0] = ldw(t0 + (i + 1) * 4096); f[(i + 1) & 1][1] = ldw(t1 + (i + 1) * 4096); }
                a2[0] = mfma(f[i & 1][0], hfr[i], a2[0]); a2[1] = mfma(f[i & 1][1], hfr[i], a2[1]);
                PIN(a2[0]); PIN(a2[1]);
                SB();
                if (i < 6) { f[(i + 1) & 1][2] = ldw(t2 + (i + 1) * 4096); f[(i + 1) & 1][3] = ldw(t3 + (i + 1) * 4096); }
                a2[2] = mfma(f[i & 1][2], hfr[i], a2[2]); a2[3] = mfma(f[i & 1][3], hfr[i], a2[3]);
                PIN(a2[2]); PIN(a2[3]);
                SB();
            }
            const u32 sc = (hf ? MR : XX) + mt * 2048 + lane * 16;
            sts128(sc, __builtin_bit_cast(u32x4, hf ? a2[0] : a2[2]));
            sts128(sc + 1024, __builtin_bit_cast(u32x4, hf ? a2[1] : a2[3]));
            SYNC(7);
            const u32 sr = (hf ? XX : MR) + mt * 2048 + lane * 16;
            u32 pkd[2][2];
#pragma unroll
            for (int t = 0; t < 2; ++t) {
                f32x4 o = __builtin_bit_cast(f32x4, lds128(sr + t * 1024));
                f32x4 s = (hf ? a2[2 + t] : a2[t]) + o;
                pkd[t][0] = san(pk(s[0] + blo(xres[t].x), s[1] + bhi(xres[t].x)));
                pkd[t][1] = san(pk(s[2] + blo(xres[t].y), s[3] + bhi(xres[t].y)));
            }
            stg128(rWS, x1o, swap_units(pkd[0][0], pkd[0][1], pkd[1][0], pkd[1][1]));
        }
        const u32 wsw = ((myrow >> 1) & 1) | (((myrow >> 3) & 1) << 1);
        const u32 wrb = E2 + myrow * 128 + (g >> 1) * 16 + (g & 1) * 8;
        proj7<7>(E3 + ta0, E3 + ta1, xf, ak);
        {
            u32x4 pos, neg;
            features(ak, wpf, g, pos, neg);
            *(LAS u32x2*)(wrb + hf * 8192 + ((0 ^ wsw) << 5)) = u32x2{pos.x, pos.y};
            *(LAS u32x2*)(wrb + hf * 8192 + ((1 ^ wsw) << 5)) = u32x2{pos.z, pos.w};
            *(LAS u32x2*)(wrb + hf * 8192 + ((2 ^ wsw) << 5)) = u32x2{neg.x, neg.y};
            *(LAS u32x2*)(wrb + hf * 8192 + ((3 ^ wsw) << 5)) = u32x2{neg.z, neg.w};
        }
        SYNC(8);
        proj7<7>(E4 + ta0, E4 + ta1, xf, av);
        {
            u32x4 vv = pk8(av[0], av[1]);
            if (g == 3) { vv.z = 0x00003f80u; vv.w = 0; }
            *(LAS u32x2*)(wrb + 16384 + (((2 * hf) ^ wsw) << 5)) = u32x2{vv.x, vv.y};
            *(LAS u32x2*)(wrb + 16384 + (((2 * hf + 1) ^ wsw) << 5)) = u32x2{vv.z, vv.w};
        }
        SYNC(9);
        {
            f32x4 kv[2] = {};
            const u32 pa = E2 + hf * 8192 + TRA(2 * (mt >> 1)), pb = E2 + hf * 8192 + TRA(2 * (mt >> 1) + 1), va = E2 + 16384 + TRA(2 * hf + (mt & 1));
#pragma unroll
            for (int ks = 0; ks < 2; ++ks) {
                const u32x4 vf = ldw(va + ks * 4096);
                kv[0] = mfma(ldw(pa + ks * 4096), vf, kv[0]);
                kv[1] = mfma(ldw(pb + ks * 4096), vf, kv[1]);
            }
            stg128(rWS, WS_KVP + ((2 * j + hf) * 32 + rt) * 4096 + mt * 1024 + lane * 16, pk8(kv[0], kv[1]));
        }
        u32x4 phq[2];
        {
            f32x4 aq[2] = {};
            proj7<0>(E0 + ta0, E0 + ta1, xf, aq);
            proj7<7>(E1 + ta0, E1 + ta1, xf, aq);
            u32x4 xv[7];
            const u32 x1off = WS_X1 + rt * 57344 + ((mt * 14 + 7 * hf) * 64 + lane) * 16;
#pragma unroll
            for (int i = 0; i < 7; ++i) xv[i] = ldg128(rWS, x1off + i * 1024);
            WAITV(7);
            SYNC(10);
            features(aq, wpf, g, phq[0], phq[1]);
            SB();
            _Pragma("unroll 1") for (u32 sp = 0; sp < SPIN_CAP && has_empty(xv); ++sp) {
                SLEEP(4);
#pragma unroll
                for (int i = 0; i < 7; ++i) xv[i] = ldg128(rWS, x1off + i * 1024);
            }
            const u32 dst = E0 + ((mt * 14 + 7 * hf) * 64 + lane) * 16;
#pragma unroll
            for (int i = 0; i < 7; ++i) sts128(dst + i * 1024, xv[i]);
        }
        SYNC(12);
        u32 ln = lane; PIN(ln);
        const u32 g2 = ln >> 4, c2 = ln & 15;
        const u32 trs2 = ((ln >> 3) & 1) | ((g2 & 1) << 1);
        const u32 trb2 = (8 * g2 + (c2 >> 2)) * 128 + (ln & 3) * 8;
#define TRB(t) (trb2 + (((t) ^ trs2) << 5))
        const u32 xb = E0 + ln * 16;
        if (w < 7) {
            f32x4 a3[4][2] = {}, a3s[2] = {};
            u32 wa[2][2];
            if (w < 6) { wa[0][0] = (w >> 1) * 4096 + TRB(2 * (w & 1)); wa[0][1] = (w >> 1) * 4096 + TRB(2 * (w & 1) + 1); wa[1][0] = wa[0][0] + 3 * 4096; wa[1][1] = wa[0][1] + 3 * 4096; }
            else { wa[0][0] = 6 * 4096 + TRB(0); wa[0][1] = 6 * 4096 + TRB(1); wa[1][0] = 6 * 4096 + TRB(2); wa[1][1] = 6 * 4096 + TRB(3); }
            u32x4 f[2][2], xr[2][4];
            wa[0][0] += E2; wa[0][1] += E2; wa[1][0] += E2; wa[1][1] += E2;
            PIN(wa[0][0]); PIN(wa[0][1]); PIN(wa[1][0]); PIN(wa[1][1]);
#define S3_EB(ks) ((((ks) >> 1) % 3 == 0) ? E3 - E2 : ((((ks) >> 1) % 3 == 1) ? E4 - E2 : 0))
#define S3_RW(ks, t) f[(ks) & 1][t] = ldw(S3_EB(ks) + wa[(ks) & 1][t])
#define S3_RX(ks, m) xr[(ks) & 1][m] = lds128(xb + ((m) * 14 + (ks)) * 1024)
#define S3_MM(ks, m) do { a3[m][0] = mfma(f[(ks) & 1][0], xr[(ks) & 1][m], a3[m][0]); a3[m][1] = mfma(f[(ks) & 1][1], xr[(ks) & 1][m], a3[m][1]); PIN(a3[m][0]); PIN(a3[m][1]); SB(); } while (0)
#define S3_MS(ks) do { a3s[0] = mfma(f[(ks) & 1][0], ONES, a3s[0]); a3s[1] = mfma(f[(ks) & 1][1], ONES, a3s[1]); PIN(a3s[0]); PIN(a3s[1]); SB(); } while (0)
#define S3_STEP(ks, rd) do { if (rd) { S3_RW((ks) + 1, 0); } S3_MS(ks); if (rd) { S3_RW((ks) + 1, 1); } S3_MM(ks, 0); \
                             if (rd) { S3_RX((ks) + 1, 0); S3_RX((ks) + 1, 1); } S3_MM(ks, 1); if (rd) { S3_RX((ks) + 1, 2); S3_RX((ks) + 1, 3); } S3_MM(ks, 2); S3_MM(ks, 3); } while (0)
            S3_RW(0, 0); S3_RW(0, 1); S3_RX(0, 0); S3_RX(0, 1); S3_RX(0, 2); S3_RX(0, 3);
            SB();
#pragma unroll
            for (int i = 0; i < 7; ++i) {
                S3_STEP(2 * i, true);
                SYNC(13 + i);
                S3_STEP(2 * i + 1, i < 6);
            }
            float mean[4], k1[4], k2[4], so[4];
#pragma unroll
            for (int m = 0; m < 4; ++m) {
                const u32 row = 16 * m + c2;
                mean[m] = ldsf(L_SUM1 + row * 4) * (1.f / DM);
                const float var = fmaxf(ldsf(L_SQ1 + row * 4) * (1.f / DM) - mean[m] * mean[m], 0.f);
                const float rstd = __builtin_amdgcn_rsqf(var + 1e-5f);
                k1[m] = -GC1 * rstd; k2[m] = -GC2 * rstd * rstd * rstd; so[m] = rstd * (1.f - ldsf(L_GATE + row * 4));
            }
#pragma unroll
            for (int m = 0; m < 4; ++m) {
                u32x4 o;
#pragma unroll
                for (int t = 0; t < 2; ++t) {
                    f32x2 y0 = f32x2{a3[m][t][0], a3[m][t][1]} - mean[m] * f32x2{a3s[t][0], a3s[t][1]};
                    f32x2 y1 = f32x2{a3[m][t][2], a3[m][t][3]} - mean[m] * f32x2{a3s[t][2], a3s[t][3]};
                    o[2 * t] = pk2(gelu2(y0, k1[m], k2[m], so[m])); o[2 * t + 1] = pk2(gelu2(y1, k1[m], k2[m], so[m]));
                }
                sts128(E1 + ((m * 7 + w) * 64 + ln) * 16, o);
            }
        } else {
            f32x4 s1[4] = {}, s2[4] = {};
            u32x4 v[2][8];
#pragma unroll
            for (int q = 0; q < 8; ++q) v[0][q] = lds128(xb + ((q >> 1) * 14 + (q & 1)) * 1024);
            SB();
#pragma unroll
            for (int i = 0; i < 7; ++i) {
                if (i < 6) {
#pragma unroll
                    for (int q = 0; q < 8; ++q) v[(i + 1) & 1][q] = lds128(xb + ((q >> 1) * 14 + 2 * (i + 1) + (q & 1)) * 1024);
                }
                SB();
#pragma unroll
                for (int q = 0; q < 8; ++q) { s1[q >> 1] = mfma(ONES, v[i & 1][q], s1[q >> 1]); s2[q >> 1] = mfma(v[i & 1][q], v[i & 1][q], s2[q >> 1]); }
#pragma unroll
                for (int m = 0; m < 4; ++m) { PIN(s1[m]); PIN(s2[m]); }
                if (i == 6) {
                    if (g2 == (c2 >> 2)) {
#pragma unroll
                        for (int m = 0; m < 4; ++m) { stsf(L_SUM1 + (16 * m + c2) * 4, s1[m][0]); stsf(L_SQ1 + (16 * m + c2) * 4, diag4(s2[m], c2)); }
                    }
                }
                SB();
                SYNC(13 + i);
            }
        }
        SYNC(20);
        const u32 mh4 = w & 1, nt4 = w >> 1;
        u32x4 af[2][9];
        {
            u32x4 kf[4];
#pragma unroll
            for (int i = 0; i < 4; ++i) kf[i] = lds128(MR + (hf * 4 + i) * 1024 + ln * 16);
            const float gt = ldsf(L_GATE + (16 * mt + c2) * 4);
#pragma unroll
            for (int m = 0; m < 2; ++m)
#pragma unroll
                for (int k = 0; k < 7; ++k) af[m][k] = lds128(E1 + (((2 * mh4 + m) * 7 + k) * 64 + ln) * 16);
            f32x4 o0 = mfma(kf[0], phq[0], f32x4{}); o0 = mfma(kf[2], phq[1], o0);
            f32x4 o1 = mfma(kf[1], phq[0], f32x4{}); o1 = mfma(kf[3], phq[1], o1);
            const float den = xg_sum(g2 == 3 ? o1[0] : 0.f) + 1e-6f;
            const float sc = gt * __builtin_amdgcn_rcpf(den);
            u32x4 f = pk8(o0 * sc, o1 * sc);
            if (g2 == 3) { f.z = 0; f.w = 0; }
            sts128(XX + ((mt * 2 + hf) * 64 + ln) * 16, f);
        }
        {
            u32 wl4 = TRB(nt4), wm4 = wl4 + E2, wh4 = wl4 + E4;
            PIN(wl4); PIN(wm4); PIN(wh4);
            const u32 row = 16 * (2 * mh4 + (g2 & 1)) + c2;
            const u32 rsoff = WS_RS + ((rt * 8 + (row >> 3)) * 8 + j) * 7168 + (nt4 * 8 + (row & 7)) * 32 + (g2 >> 1) * 16;
            u32x4 f[2][7];
#define S4_ENT(cb) (((cb) % 3 == 0) ? wh4 : (((cb) % 3 == 1) ? wm4 : wl4 + E1))
#define WO_ADDR(u) ((u) < 7 ? wl4 + E0 + (u) * 4096 : wm4 + (E3 - E2) + ((u) - 7) * 4096)
#pragma unroll
            for (int cb = 0; cb < 8; ++cb) {
                if (cb < 7) SYNC(21 + cb);
                if (cb == 0) {
#pragma unroll
                    for (int k = 0; k < 7; ++k) f[0][k] = ldw(S4_ENT(0) + k * 4096);
#pragma unroll
                    for (int m = 0; m < 2; ++m)
#pragma unroll
                        for (int k = 0; k < 2; ++k) af[m][7 + k] = lds128(XX + (((2 * mh4 + m) * 2 + k) * 64 + ln) * 16);
                } else {
                    const int p = cb - 1;
                    f32x4 a4[2] = {};
                    const u32x4 wo0 = ldw(WO_ADDR(2 * p)), wo1 = ldw(WO_ADDR(2 * p + 1));
#pragma unroll
                    for (int k = 0; k < 7; ++k) {
                        if (cb < 7) f[cb & 1][k] = ldw(S4_ENT(cb) + k * 4096);
                        a4[0] = mfma(f[p & 1][k], af[0][k], a4[0]);
                        a4[1] = mfma(f[p & 1][k], af[1][k], a4[1]);
                        PIN(a4[0]); PIN(a4[1]);
                        SB();
                    }
                    a4[0] = mfma(wo0, af[0][7], a4[0]); a4[1] = mfma(wo0, af[1][7], a4[1]);
                    a4[0] = mfma(wo1, af[0][8], a4[0]); a4[1] = mfma(wo1, af[1][8], a4[1]);
                    u32x4 u = swap_units(pk(a4[0][0], a4[0][1]), pk(a4[0][2], a4[0][3]), pk(a4[1][0], a4[1][1]), pk(a4[1][2], a4[1][3]));
                    stg128(rWS, rsoff + p * 1024, u);
                    SB();
                }
            }
        }
        SYNCW(0, 28);
        SYNC(29);
        if (tid < 448) {
            const u32 off = WS_RS + ((rt * 8 + j) * 8) * 7168 + tid * 16;
            u32x4 p[8];
#pragma unroll
            for (int s = 0; s < 8; ++s) p[s] = ldg128(rWS, off + s * 7168);
            const u32 half = tid & 1, r = (tid >> 1) & 7, nn = tid >> 4;
            const u32 row = 8 * j + r, kg = 2 * nn + half;
            const u32x4 x1res = ldg128(rWS, WS_X1 + rt * 57344 + (((row >> 4) * 14 + (kg >> 2)) * 64 + (kg & 3) * 16 + (row & 15)) * 16);
            const float og = 1.f - ldsf(L_GATE + (8 * j + r) * 4);
            u32x4 o;
#pragma unroll
            for (int e = 0; e < 4; ++e) {
                f32x2 a = f32x2{blo(x1res[e]), bhi(x1res[e])} * og;
#pragma unroll
                for (int s = 0; s < 8; ++s) a += f32x2{blo(p[s][e]), bhi(p[s][e])};
                o[e] = pk2(a);
            }
            *(GAS u32x4*)((GAS unsigned char*)out + (size_t)(row0 + 8 * j + r) * 896 + nn * 32 + half * 16) = o;
        }
    }
}
}

extern "C" void kernel_launch(void* const* d_in, const int* in_sizes, int n_in, void* d_out, int out_size, void* d_ws, size_t ws_size,
                              hipStream_t stream) {
    (void)hipFuncSetAttribute((const void*)fused_kernel, hipFuncAttributeMaxDynamicSharedMemorySize, LDS_BYTES);
    fused_kernel<<<256, NWAVE * 64, LDS_BYTES, stream>>>((const u16*)d_in[0], (const u16*)d_in[1], (const u16*)d_in[2], (const u16*)d_in[3],
                                                         (const u16*)d_in[5], (const u16*)d_in[6], (const u16*)d_in[7], (const u16*)d_in[8],
                                                         (const u16*)d_in[9], (u16*)d_out, (unsigned char*)d_ws);
}
```

```cpp
#include <hip/hip_runtime.h>
#include <hip/hip_bf16.h>

namespace {
typedef unsigned short u16;
typedef unsigned u32;
typedef unsigned long long u64;
typedef short bf16x8 __attribute__((ext_vector_type(8)));
typedef short s16x4 __attribute__((ext_vector_type(4)));
typedef float f32x4 __attribute__((ext_vector_type(4)));
typedef float f32x2 __attribute__((ext_vector_type(2)));
typedef __bf16 bf16x2 __attribute__((ext_vector_type(2)));
typedef u32 u32x4 __attribute__((ext_vector_type(4)));
typedef u32 u32x2 __attribute__((ext_vector_type(2)));
#define LAS __attribute__((address_space(3)))
#define GAS __attribute__((address_space(1)))
#define DI __device__ __forceinline__
typedef __amdgpu_buffer_rsrc_t rsrc_t;

constexpr int SEQ = 2048, DM = 448, FF = 1792;

constexpr u32 E0 = 0, E1 = 28672, E2 = 57344, E3 = 86016, E4 = 114688, XX = 143360, MR = 151552, MISC = 159744;
constexpr u32 LDS_BYTES = 163840;
constexpr u32 L_SUM = MISC, L_SQ = MISC + 256, L_GATE = MISC + 512, L_SUM1 = MISC + 768, L_SQ1 = MISC + 1024, L_CS1 = MISC + 3072, GWL = MISC + 2048;
constexpr u32 WPHI = MR;
constexpr u32 XT_CB = 8192;

constexpr u32 WS_EPOCH = 0;
constexpr u32 WS_FCLR = 4096;
constexpr u32 WS_FKV = 12288;
constexpr u32 WS_FRS = 16384;
constexpr u32 WS_H = 65536;
constexpr u32 WS_X1 = WS_H + 32 * 57344;
constexpr u32 WS_KVP = WS_X1 + 32 * 57344;
constexpr u32 WS_KVF = WS_KVP + 16 * 32 * 8192;
constexpr u32 WS_RS = WS_KVF + 16 * 4096;
constexpr u32 WS_END = WS_RS + 32 * 64 * 7168;
constexpr u32 SPIN_CAP = 1u << 16;
constexpr int SC1 = 16;
constexpr u32 CODE_BYTES = 36864;

DI f32x4 mfma(u32x4 a, u32x4 b, f32x4 c) {
    return __builtin_amdgcn_mfma_f32_16x16x32_bf16(__builtin_bit_cast(bf16x8, a), __builtin_bit_cast(bf16x8, b), c, 0, 0, 0);
}
DI u32 pk(float a, float b) { f32x2 f = {a, b}; return __builtin_bit_cast(u32, __builtin_convertvector(f, bf16x2)); }
DI u32 pk2(f32x2 f) { return __builtin_bit_cast(u32, __builtin_convertvector(f, bf16x2)); }
DI u32x4 pk8(f32x4 a, f32x4 b) { return u32x4{pk(a[0], a[1]), pk(a[2], a[3]), pk(b[0], b[1]), pk(b[2], b[3])}; }
DI float blo(u32 v) { return __builtin_bit_cast(float, v << 16); }
DI float bhi(u32 v) { return __builtin_bit_cast(float, v & 0xffff0000u); }
DI u32x4 lds128(u32 a) { return *(LAS const u32x4*)a; }
DI u32x2 lds64(u32 a) { return *(LAS const u32x2*)a; }
DI float ldsf(u32 a) { return *(LAS const float*)a; }
DI void sts128(u32 a, u32x4 v) { *(LAS u32x4*)a = v; }
DI void stsf(u32 a, float v) { *(LAS float*)a = v; }
DI u32x4 ldw2(u32 a0, u32 a1) {
    s16x4 lo = __builtin_amdgcn_ds_read_tr16_b64_v4i16((LAS s16x4*)a0);
    s16x4 hi = __builtin_amdgcn_ds_read_tr16_b64_v4i16((LAS s16x4*)a1);
    u32x2 l = __builtin_bit_cast(u32x2, lo), h = __builtin_bit_cast(u32x2, hi);
    return u32x4{l.x, l.y, h.x, h.y};
}
DI u32x4 ldw(u32 a) { return ldw2(a, a + 512); }
DI void dma16(rsrc_t rs, u32 voff, u32 soff, u32 lds) {
    asm volatile("s_mov_b32 m0, %0\n\ts_nop 0\n\tbuffer_load_dwordx4 %1, %2, %3 offen lds" ::"s"(lds), "v"(voff), "s"(rs), "s"(soff) : "memory");
}
DI void dma_pair(rsrc_t rs, u32 v0, u32 v1, u32 soff, u32 lds) {
    asm volatile("s_mov_b32 m0, %0\n\ts_nop 0\n\t"
                 "buffer_load_dwordx4 %1, %3, %4 offen lds\n\t"
                 "buffer_load_dwordx4 %2, %3, %4 offen offset:1024 lds"
                 ::"s"(lds), "v"(v0), "v"(v1), "s"(rs), "s"(soff) : "memory");
}
struct Pat { u32 v[4]; };
DI Pat mkpat(u32 a, u32 b, u32 step) { Pat p; p.v[0] = a; p.v[1] = b + (step - 1024); p.v[2] = a + 2 * (step - 1024); p.v[3] = b + 3 * (step - 1024); return p; }
DI void dma_unit(rsrc_t rs, const Pat& p, u32 soff, u32 lds) {
    asm volatile("s_mov_b32 m0, %0\n\ts_nop 0\n\t"
                 "buffer_load_dwordx4 %1, %5, %6 offen lds\n\t"
                 "buffer_load_dwordx4 %2, %5, %6 offen offset:1024 lds\n\t"
                 "buffer_load_dwordx4 %3, %5, %6 offen offset:2048 lds\n\t"
                 "buffer_load_dwordx4 %4, %5, %6 offen offset:3072 lds"
                 ::"s"(lds), "v"(p.v[0]), "v"(p.v[1]), "v"(p.v[2]), "v"(p.v[3]), "s"(rs), "s"(soff) : "memory");
}
#define WAITV(n) asm volatile("s_waitcnt vmcnt(" #n ")" ::: "memory")
#define SLEEP(n) asm volatile("s_sleep " #n ::: "memory")
#define BAR() asm volatile("s_waitcnt lgkmcnt(0)\n\ts_barrier" ::: "memory")
#define SB() __builtin_amdgcn_sched_barrier(0)
DI u32x4 ldg128(rsrc_t rs, u32 off) { return __builtin_amdgcn_raw_buffer_load_b128(rs, off, 0, SC1); }
DI u32x2 ldg64(rsrc_t rs, u32 off) { return __builtin_amdgcn_raw_buffer_load_b64(rs, off, 0, SC1); }
DI void stg128(rsrc_t rs, u32 off, u32x4 v) { __builtin_amdgcn_raw_buffer_store_b128(v, rs, off, 0, SC1); }
DI void stg64(rsrc_t rs, u32 off, u32x2 v) { __builtin_amdgcn_raw_buffer_store_b64(v, rs, off, 0, SC1); }
DI u32 san(u32 v) { return v < 0xfffffffeu ? v : 0xfffffffeu; }
DI u32 umax3(u32 a, u32 b, u32 c) { u32 m = a > b ? a : b; return m > c ? m : c; }
DI u32 fmax4(u32 m, u32x4 v) { return umax3(umax3(m, v.x, v.y), v.z, v.w); }
template <int N> DI bool has_empty(const u32x4 (&f)[N]) {
    u32 m = 0;
#pragma unroll
    for (int i = 0; i < N; ++i) m = fmax4(m, f[i]);
    return __any(m == 0xffffffffu);
}
DI f32x2 gelu2(f32x2 y, float k1, float k2, float so) {
    f32x2 t = __builtin_elementwise_fma(y * y, f32x2{k2, k2}, f32x2{k1, k1}) * y;
    f32x2 d = f32x2{__builtin_amdgcn_exp2f(t.x), __builtin_amdgcn_exp2f(t.y)} + 1.f;
    f32x2 r = {__builtin_amdgcn_rcpf(d.x), __builtin_amdgcn_rcpf(d.y)};
    return (y * so) * r;
}
constexpr float GC1 = 2.3022082f, GC2 = 0.10294324f;
DI float f_of(u32 v) { return __builtin_bit_cast(float, v); }
DI u32 u_of(float v) { return __builtin_bit_cast(u32, v); }
DI float xg_sum(float v) {
    auto r = __builtin_amdgcn_permlane32_swap(u_of(v), u_of(v), false, false);
    v = f_of(r[0]) + f_of(r[1]);
    auto s = __builtin_amdgcn_permlane16_swap(u_of(v), u_of(v), false, false);
    return f_of(s[0]) + f_of(s[1]);
}
DI float xg_max(float v) {
    auto r = __builtin_amdgcn_permlane32_swap(u_of(v), u_of(v), false, false);
    v = fmaxf(f_of(r[0]), f_of(r[1]));
    auto s = __builtin_amdgcn_permlane16_swap(u_of(v), u_of(v), false, false);
    return fmaxf(f_of(s[0]), f_of(s[1]));
}
template <int CTRL> DI float dpp(float v) {
    return __builtin_bit_cast(float, __builtin_amdgcn_update_dpp(0, __builtin_bit_cast(int, v), CTRL, 0xf, 0xf, true));
}
DI float row_sum(float v) { v += dpp<0x128>(v); v += dpp<0x124>(v); v += dpp<0x122>(v); v += dpp<0x121>(v); return v; }
DI float row_max(float v) {
    asm volatile("s_nop 1\n\tv_max_f32_dpp %0, %0, %0 row_ror:8 row_mask:0xf bank_mask:0xf\n\ts_nop 1\n\t"
                 "v_max_f32_dpp %0, %0, %0 row_ror:4 row_mask:0xf bank_mask:0xf\n\ts_nop 1\n\t"
                 "v_max_f32_dpp %0, %0, %0 row_ror:2 row_mask:0xf bank_mask:0xf\n\ts_nop 1\n\t"
                 "v_max_f32_dpp %0, %0, %0 row_ror:1 row_mask:0xf bank_mask:0xf" : "+v"(v));
    return v;
}
DI u32x4 swap_units(u32 a0, u32 a1, u32 b0, u32 b1) {
    auto r0 = __builtin_amdgcn_permlane16_swap(a0, b0, false, false);
    auto r1 = __builtin_amdgcn_permlane16_swap(a1, b1, false, false);
    return u32x4{(u32)r0[0], (u32)r1[0], (u32)r0[1], (u32)r1[1]};
}
constexpr float LOG2E = 1.4426950408889634f;
DI float diag4(f32x4 d, u32 c) { float a = (c & 1) ? d[1] : d[0], b = (c & 1) ? d[3] : d[2]; return (c & 2) ? b : a; }

#define PTA(id)
#define PTB(id)

#define PIN(x) asm volatile("" : "+v"(x))
constexpr int NWAVE = 12;
constexpr u32 L_PROG = MISC + 1280, L_HELP = L_PROG + 32, L_AUX = L_PROG + 36, L_LAND = L_PROG + 40, L_DONE = L_PROG + 48;
DI void sigv(u32 addr, u32 val) { asm volatile("ds_write_b32 %0, %1" ::"v"(addr), "v"(val) : "memory"); }
DI void sig(u32 addr, u32 val) { asm volatile("s_mov_b64 exec, 1\n\tds_write_b32 %0, %1\n\ts_mov_b64 exec, -1" ::"v"(addr), "v"(val) : "memory"); }
DI u32 peek(u32 addr) { return *(volatile LAS u32*)addr; }
DI void wait_ge(u32 addr, u32 thr) {
    _Pragma("unroll 1") for (u32 sp = 0; sp < (SPIN_CAP << 4); ++sp) {
        u32 v;
        asm volatile("ds_read_b32 %0, %1\n\ts_waitcnt lgkmcnt(0)" : "=v"(v) : "v"(addr) : "memory");
        if (__all(v >= thr)) break;
        SLEEP(1);
    }
}

DI u32 rl(u32 v, int l) { return __builtin_amdgcn_readlane(v, l); }
DI void snap_wait(u32 aS, u32 thr, u32& sv) {
    _Pragma("unroll 1") for (u32 sp = 0; sp < (SPIN_CAP << 4); ++sp) {
        asm volatile("ds_read_b32 %0, %1\n\ts_waitcnt lgkmcnt(0)" : "=v"(sv) : "v"(aS) : "memory");
        if (__all(sv >= thr)) break;
        SLEEP(1);
    }
}
template <int K0, bool CS = false>
DI void proj7(u32 wa0, u32 wa1, const u32x4 (&xf)[14], f32x4 (&acc)[2], u32 aS, u32& sv, u32 mt = 0, f32x4* cs = nullptr) {
    u32x4 f[4][2];
    PIN(wa0); PIN(wa1);
#pragma unroll
    for (int k = 0; k < 3; ++k) { f[k][0] = ldw(wa0 + k * 4096); f[k][1] = ldw(wa1 + k * 4096); }
    SB();
#pragma unroll
    for (int k = 0; k < 7; ++k) {
        if (k + 3 < 7) { f[(k + 3) & 3][0] = ldw(wa0 + (k + 3) * 4096); f[(k + 3) & 3][1] = ldw(wa1 + (k + 3) * 4096); }
        if (k == 4) sv = peek(aS);
        acc[0] = mfma(f[k & 3][0], xf[K0 + k], acc[0]);
        acc[1] = mfma(f[k & 3][1], xf[K0 + k], acc[1]);
        PIN(acc[0]); PIN(acc[1]);
        if (CS && ((K0 + k) & 3) == mt) {
            const u32x4 ones = {0x3f803f80u, 0x3f803f80u, 0x3f803f80u, 0x3f803f80u};
            cs[0] = mfma(f[k & 3][0], ones, cs[0]); cs[1] = mfma(f[k & 3][1], ones, cs[1]);
        }
        SB();
    }
}

DI void features(const f32x4 (&a)[2], const u32x4 (&wpf)[2], u32 g, u32x4& pos, u32x4& neg) {
    u32x4 qf = pk8(a[0], a[1]);
    if (g == 3) { qf.z = 0; qf.w = 0; }
    f32x4 p0 = mfma(wpf[0], qf, f32x4{}) * LOG2E, p1 = mfma(wpf[1], qf, f32x4{}) * LOG2E;
    const bool v1 = g < 3;
    if (!v1) p1 = p0;
    float mx = fmaxf(fmaxf(fmaxf(p0[0], p0[1]), fmaxf(p0[2], p0[3])), fmaxf(fmaxf(p1[0], p1[1]), fmaxf(p1[2], p1[3])));
    float mn = fminf(fminf(fminf(p0[0], p0[1]), fminf(p0[2], p0[3])), fminf(fminf(p1[0], p1[1]), fminf(p1[2], p1[3])));
    mx = xg_max(mx); mn = -xg_max(-mn);
    const f32x4 d0 = p0 - mx, d1 = p1 - mx, e0 = mn - p0, e1 = mn - p1;
    f32x4 a0, a1, b0, b1;
#pragma unroll
    for (int r = 0; r < 4; ++r) {
        a0[r] = __builtin_amdgcn_exp2f(d0[r]); a1[r] = __builtin_amdgcn_exp2f(d1[r]);
        b0[r] = __builtin_amdgcn_exp2f(e0[r]); b1[r] = __builtin_amdgcn_exp2f(e1[r]);
    }
    if (!v1) { a1 = f32x4{}; b1 = f32x4{}; }
    const f32x4 sa4 = a0 + a1, sb4 = b0 + b1;
    const float ra = __builtin_amdgcn_rcpf(xg_sum((sa4[0] + sa4[1]) + (sa4[2] + sa4[3])));
    const float rb = __builtin_amdgcn_rcpf(xg_sum((sb4[0] + sb4[1]) + (sb4[2] + sb4[3])));
    pos = pk8(a0 * ra, a1 * ra);
    neg = pk8(b0 * rb, b1 * rb);
}

__global__ void __launch_bounds__(NWAVE * 64, 3)
fused_kernel(const u16* __restrict__ x, const u16* __restrict__ wqkv, const u16* __restrict__ wphi, const u16* __restrict__ wo,
             const u16* __restrict__ w1, const u16* __restrict__ w2, const u16* __restrict__ w3, const u16* __restrict__ w4,
             const u16* __restrict__ gw, u16* out, unsigned char* ws) {
    extern __shared__ __attribute__((aligned(16))) unsigned char smem[];
    const u32 tid = threadIdx.x, lane = tid & 63;
    const u32 w = __builtin_amdgcn_readfirstlane(tid >> 6);
#define TOUCH(rs, off) asm volatile("s_mov_b32 m0, %0\n\ts_nop 0\n\tbuffer_load_dword %1, %2, 0 offen lds" ::"s"(MISC + 1536), "v"(off), "s"(rs) : "memory")
    if (w == 7) {
        u64 pc; int n0;
        asm volatile("s_getpc_b64 %0\n.Lfk_pc:\n\ts_mov_b32 %1, 0\n\ts_max_i32 %1, %1, .Lfk_e0-.Lfk_pc\n\ts_max_i32 %1, %1, .Lfk_e1-.Lfk_pc\n\ts_max_i32 %1, %1, .Lfk_e2-.Lfk_pc"
                     : "=s"(pc), "=s"(n0) :: "scc");
        const rsrc_t rC = __builtin_amdgcn_make_buffer_rsrc((void*)pc, 0, n0, 0x00020000);
        TOUCH(rC, lane < 12 ? ((blockIdx.x >> 3) + lane * 32) * 128 : 0x40000000u);
    }
    const u32 g = lane >> 4, c = lane & 15;
    const u32 b = blockIdx.x;
    const u32 rt = 8 * ((b & 7) >> 1) + (b >> 5), j = 4 * (b & 1) + ((b >> 3) & 3);
    const int row0 = rt * 64;
    PTA(0);
    const rsrc_t rWS = __builtin_amdgcn_make_buffer_rsrc((void*)ws, 0, WS_END, 0x00020000);
    const bool hasB = j < 7;
    const u32 psnS = hasB ? 0u : 0x40000000u;
    const u32 odd = j & 1;
#define DEF_TR() u32 lt_ = lane; PIN(lt_); const u32 q4 = (lt_ & 15) >> 2, pp = lt_ & 3; \
    const u32 trs = ((q4 >> 1) & 1) | (((lt_ >> 4) & 1) << 1), trb = (8 * (lt_ >> 4) + q4) * 128 + pp * 8
#define TRA(t) (trb + (((t) ^ trs) << 5))
    const u32x4 ONES = {0x3f803f80u, 0x3f803f80u, 0x3f803f80u, 0x3f803f80u};
    const u32x4 EMPTY = {0xffffffffu, 0xffffffffu, 0xffffffffu, 0xffffffffu};
#define LT(n) ((u32)(7 * ((n) + 1)))
    const u32 aC = L_PROG + (lane & 7) * 4;
    if (w == 10) sigv(L_PROG + (lane & 31) * 4, (lane & 31) >= 16 ? 98 : ((lane & 31) >= 10 && (lane & 31) < 13 ? (lane & 31) - 10 : 0));
    const bool isLoader = w >= 8 && w != 10;
    if (!isLoader) BAR();

    auto finish_block = [&](u32 p) -> bool {
        const u32 t = p * 64 + lane;
        const u32 off = WS_RS + ((rt * 8 + j) * 8) * 7168 + t * 16;
        u32x4 q[8];
#pragma unroll
        for (int s = 0; s < 8; ++s) q[s] = ldg128(rWS, off + s * 7168);
        const u32 half = t & 1, r = (t >> 1) & 7, nn = t >> 4;
        const u32 row = 8 * j + r, kg = 2 * nn + half;
        const u32x4 x1res = ldg128(rWS, WS_X1 + rt * 57344 + (((row >> 4) * 14 + (kg >> 2)) * 64 + (kg & 3) * 16 + (row & 15)) * 16);
        const float og = 1.f - ldsf(L_GATE + row * 4);
        u32x4 o;
#pragma unroll
        for (int e = 0; e < 4; ++e) {
            f32x2 a = f32x2{blo(x1res[e]), bhi(x1res[e])} * og;
#pragma unroll
            for (int s = 0; s < 8; ++s) a += f32x2{blo(q[s][e]), bhi(q[s][e])};
            o[e] = pk2(a);
        }
        *(GAS u32x4*)((GAS unsigned char*)out + (size_t)(row0 + row) * 896 + nn * 32 + half * 16) = o;
        return true;
    };

    if (isLoader) {
        const rsrc_t rX = __builtin_amdgcn_make_buffer_rsrc((void*)x, 0, SEQ * DM * 2, 0x00020000);
        const rsrc_t rQKV = __builtin_amdgcn_make_buffer_rsrc((void*)wqkv, 0, DM * 3 * DM * 2, 0x00020000);
        const rsrc_t rWO = __builtin_amdgcn_make_buffer_rsrc((void*)wo, 0, DM * DM * 2, 0x00020000);
        const rsrc_t rW1 = __builtin_amdgcn_make_buffer_rsrc((void*)w1, 0, DM * DM * 2, 0x00020000);
        const rsrc_t rW2 = __builtin_amdgcn_make_buffer_rsrc((void*)w2, 0, DM * DM * 2, 0x00020000);
        const rsrc_t rW3 = __builtin_amdgcn_make_buffer_rsrc((void*)w3, 0, DM * FF * 2, 0x00020000);
        const rsrc_t rW4 = __builtin_amdgcn_make_buffer_rsrc((void*)w4, 0, DM * FF * 2, 0x00020000);
        const u32 l3 = w == 11 ? 2 : w - 8;
        u32 myL = L_LAND + l3 * 4, three = 3;
        PIN(myL); PIN(three);
        const u32 r8 = lane >> 3, sl = lane & 7;
        const u32 chA = sl ^ (((r8 >> 1) & 1) << 1), chB = chA ^ 4;
        const u32 jb = hasB ? j : 0;
        u32 rot = l3, pend = 0, seen = 0;
#define LANDED() asm volatile("s_mov_b64 exec, 1\n\tds_add_u32 %0, %1\n\ts_mov_b64 exec, -1" ::"v"(myL), "v"(three) : "memory")
#define POLL() do { u32 st_; asm volatile("s_getreg_b32 %0, hwreg(HW_REG_IB_STS)" : "=s"(st_)); \
            const u32 left_ = (((st_ & 15u) | ((st_ >> 18) & 48u)) + 3) >> 2; \
            _Pragma("unroll 1") while (pend > left_) { LANDED(); --pend; } } while (0)
#define ISSUED() do { ++pend; POLL(); } while (0)
#define FLUSH() do { _Pragma("unroll 1") while (pend > 0) { POLL(); SLEEP(1); } } while (0)
#define ROOM(need) do { if (__builtin_expect(!__all(seen >= (u32)(need)), 0)) { \
            _Pragma("unroll 1") for (u32 sp = 0; sp < (SPIN_CAP << 4); ++sp) { seen = peek(aC); if (__all(seen >= (u32)(need))) break; POLL(); } } } while (0)
#define RUN7(k, need, rs, pat, so0, sstep, ent) do { ROOM(need); PTB(k); u32 u = rot; \
            _Pragma("unroll 1") for (; u < 7; u += 3) { dma_unit(rs, pat, (so0) + u * (sstep), (ent) + u * 4096); ISSUED(); } rot = u - 7; } while (0)
        {
            const u32 xa = (row0 + r8) * 896 + ((sl ^ (r8 >> 1)) * 16);
            const Pat pX = mkpat(xa, xa ^ 64, 8 * 896);
            u32 u = rot;
            dma_unit(rX, pX, (u >> 1) * 128 + (u & 1) * (32 * 896), u * 4096); ++pend; u += 3;
            BAR();
#pragma unroll 1
            for (; u < 14; u += 3) { dma_unit(rX, pX, (u >> 1) * 128 + (u & 1) * (32 * 896), u * 4096); ISSUED(); }
            rot = u - 14;
        }
        const Pat pS = mkpat(r8 * 896 + chA * 16, r8 * 896 + chB * 16, 8 * 896);
        const Pat pQ = mkpat(r8 * 2688 + (chA >> 2) * 56 + (chA & 3) * 16, r8 * 2688 + (chB >> 2) * 56 + (chB & 3) * 16, 8 * 2688);
#define LD_STD(k, need, rs, half, ent) RUN7(k, need, rs, pS, (224 * (half)) * 896 + jb * 128, 32 * 896, ent)
#define LD_QKV(k, need, sec, half, ent) RUN7(k, need, rQKV, pQ, (224 * (half)) * 2688 + (sec) * 896 + 112 * j, 32 * 2688, ent)
        LD_STD(1, 0, rW1, 0, E2);
        LD_STD(2, 0, rW1, 1, E3);
        LD_QKV(3, 0, 1, 0, E4);
        LD_STD(4, 1, rW2, 0, E0);
        LD_STD(5, 1, rW2, 1, E1);
        LD_QKV(6, 2, 2, 0, E2);
        LD_QKV(7, 3, 1, 1, E3);
        LD_QKV(8, 4, 2, 1, E4);
        LD_QKV(9, 5, 0, 0, E0);
        LD_QKV(10, 5, 0, 1, E1);
        const Pat pT = mkpat(r8 * 3584 + chA * 16, r8 * 3584 + chB * 16, 8 * 3584);
        const Pat pU = mkpat((r8 + (chA >> 2) * 32) * 3584 + (chA & 3) * 16, (r8 + (chB >> 2) * 32) * 3584 + (chB & 3) * 16, 8 * 3584);
        const u32 s3f = 448 * j + odd * 64, s3h = 448 * j + (odd ? 0 : 384);
        const u32 pr = 16 * (r8 >> 2) + (r8 & 3);
        const Pat pP = mkpat(pr * 896 + chA * 16, pr * 896 + chB * 16, 4 * 896);
        const u32 s4f = (224 * j + odd * 32) * 896, s4l = (224 * j + (odd ? 0 : 192)) * 896, sWo = 56 * j * 896;
        PTB(11);
        __builtin_amdgcn_s_setprio(2);
        {
            u64 tt = 0x165a098765a987ull, tn = 0xaa987765221100ull, ts = 0x70060543201ull;
            u32 so = s3f;
#pragma unroll 1
            for (u32 ks = 0; ks < 14; ++ks) {
                ROOM(7 + ((u32)tn & 15u));
                dma_unit(rW3, pT, so + rot * 128, ((u32)tt & 15u) * 12288 + rot * 4096); ISSUED();
                const u32 si = (u32)ts & 15u;
                if (si) {
                    if (rot == 0) {
                        ROOM(8 + ((0x7510000u >> (4 * (si - 1))) & 15u));
                        dma_unit(rW3, pU, s3h + (si - 1) * (64 * 3584), (u32)(0x22210e24232221ull >> (8 * (si - 1)) & 255u) * 4096); ISSUED(); rot = 2;
                    } else --rot;
                }
                tt >>= 4; tn >>= 4; ts >>= 4; so += 32 * 3584;
            }
        }
        PTB(12);
        {
            u64 tt = 0xb41635a02987ull, tn = 0xaaa655432210ull;
            const u32 o0 = s4f + rot * (32 * 896), o2 = rot == 0 ? s4l : sWo + (rot - 1) * (28 * 896);
            u32 h = 0;
#pragma unroll 1
            for (u32 cb = 0; cb < 7; ++cb) {
#pragma unroll
                for (u32 t3 = 0; t3 < 3; ++t3) {
                    ROOM(cb < 4 ? 18 + ((u32)tn & 15u) : 17 + h);
                    const u32 ld = ((u32)tt & 15u) * 12288 + rot * 4096;
                    if (t3 < 2) dma_unit(rW4, pP, o0 + t3 * (96 * 896) + cb * 128, ld);
                    else if (rot == 0) dma_unit(rW4, pP, o2 + cb * 128, ld);
                    else dma_unit(rWO, pP, o2 + cb * 128, ld);
                    ISSUED();
                    tt = (tt >> 4) | ((tt & 15ull) << 44); tn >>= 4; ++h;
                }
            }
        }
        FLUSH();
        asm volatile(".Lfk_e0:");
    } else if (w == 10) {
        const rsrc_t rPHI = __builtin_amdgcn_make_buffer_rsrc((void*)wphi, 0, 16 * 28 * 28 * 2, 0x00020000);
        const rsrc_t rGW = __builtin_amdgcn_make_buffer_rsrc((void*)gw, 0, DM * 2, 0x00020000);
        const u32 head = 2 * j + (rt >> 4), chunk = rt & 15;
        SLEEP(12);
        dma16(rGW, lane * 16, 0, GWL);
        dma_pair(rPHI, lane * 16, lane * 16, j * 3136, WPHI);
        dma_pair(rPHI, lane * 16, lane * 16, j * 3136 + 2048, WPHI + 2048);
        const u32 tag = __builtin_amdgcn_readfirstlane(__builtin_amdgcn_raw_buffer_load_b32(rWS, WS_EPOCH, 0, SC1)) + 1;
        const u32 taghi = tag * 0x9E3779B1u ^ 0x5bd1e995u;
        WAITV(0);
        sig(L_HELP, 1);
        {
            const rsrc_t rQKV = __builtin_amdgcn_make_buffer_rsrc((void*)wqkv, 0, DM * 3 * DM * 2, 0x00020000);
            const rsrc_t rWO = __builtin_amdgcn_make_buffer_rsrc((void*)wo, 0, DM * DM * 2, 0x00020000);
            const rsrc_t rW2 = __builtin_amdgcn_make_buffer_rsrc((void*)w2, 0, DM * DM * 2, 0x00020000);
            const rsrc_t rW3 = __builtin_amdgcn_make_buffer_rsrc((void*)w3, 0, DM * FF * 2, 0x00020000);
            const rsrc_t rW4 = __builtin_amdgcn_make_buffer_rsrc((void*)w4, 0, DM * FF * 2, 0x00020000);
            TOUCH(rQKV, lane < 37 ? (b * 37 + lane) * 128 : 0x40000000u);
            TOUCH(rW2, lane < 13 ? (b * 13 + lane) * 128 : 0x40000000u);
            TOUCH(rW3, lane < 49 ? (b * 49 + lane) * 128 : 0x40000000u);
            TOUCH(rW4, lane < 49 ? (b * 49 + lane) * 128 : 0x40000000u);
            TOUCH(rWO, lane < 13 ? (b * 13 + lane) * 128 : 0x40000000u);
        }
        wait_ge(aC, 2);
        if (lane == 0) stg64(rWS, WS_FCLR + rt * 128 + j * 8, u32x2{tag, taghi});
        PTA(2);
        {
            const u32 l24 = lane < 24 ? lane : 0;
            int nb = (int)rt - 1 + (int)(l24 >> 3);
            nb = nb < 0 ? 0 : (nb > 31 ? 31 : nb);
            const u32 fo = WS_FCLR + nb * 128 + (l24 & 7) * 8;
            _Pragma("unroll 1") for (u32 sp = 0; sp < SPIN_CAP; ++sp) {
                u32x2 fl = ldg64(rWS, fo);
                if (__all(fl.x == tag && fl.y == taghi)) break;
                SLEEP(2);
            }
        }
        sig(L_HELP, 100);
        PTA(3);
        if (lane < 16) stg128(rWS, WS_KVF + head * 4096 + chunk * 256 + lane * 16, EMPTY);
#pragma unroll 1
        for (u32 i = 0; i < 8; ++i) stg128(rWS, WS_KVP + ((2 * j + (i >> 2)) * 32 + rt) * 4096 + (i & 3) * 1024 + lane * 16, EMPTY);
        WAITV(0);
        sig(L_AUX, 60);
        if (lane == 0) stg64(rWS, WS_FKV + j * 256 + rt * 8, u32x2{tag, taghi});
        PTA(4);
        {
            const rsrc_t rWO = __builtin_amdgcn_make_buffer_rsrc((void*)wo, 0, DM * DM * 2, 0x00020000);
            const rsrc_t rW3 = __builtin_amdgcn_make_buffer_rsrc((void*)w3, 0, DM * FF * 2, 0x00020000);
            const rsrc_t rW4 = __builtin_amdgcn_make_buffer_rsrc((void*)w4, 0, DM * FF * 2, 0x00020000);
            const u32 e8 = rt & 7, ok = lane < 56 ? 0u : 0x40000000u;
#pragma unroll 1
            for (u32 i = 0; i < 4; ++i) { TOUCH(rW3, ((i * 112 + e8 * 14 + (lane >> 2)) * 3584 + ((448 * j) & ~127u) + (lane & 3) * 128) | ok); SLEEP(4); }
#pragma unroll 1
            for (u32 i = 0; i < 4; ++i) { TOUCH(rW4, (224 * j * 896 + (e8 * 196 + i * 49 + lane) * 128) | (lane < 49 ? 0u : 0x40000000u)); SLEEP(4); }
            TOUCH(rWO, (56 * j * 896 + (e8 * 53 + lane) * 128) | (lane < 53 ? 0u : 0x40000000u));
        }
        _Pragma("unroll 1") for (u32 sp = 0; sp < SPIN_CAP; ++sp) {
            u32x2 fl = ldg64(rWS, WS_FKV + j * 256 + (lane & 31) * 8);
            if (__all(fl.x == tag && fl.y == taghi)) break;
            SLEEP(8);
        }
        PTA(5);
        wait_ge(aC, 9);
        u32x4 pv[8];
        {
            const u32 poff = WS_KVP + (head * 32 + g) * 4096 + chunk * 256 + c * 16;
            _Pragma("unroll 1") for (u32 sp = 0; sp < SPIN_CAP; ++sp) {
#pragma unroll
                for (int i = 0; i < 8; ++i) pv[i] = ldg128(rWS, poff + i * 16384);
                if (!has_empty(pv)) break;
                SLEEP(6);
            }
            u32x4 o;
#pragma unroll
            for (int e = 0; e < 4; ++e) {
                f32x2 s = {0.f, 0.f};
#pragma unroll
                for (int i = 0; i < 8; ++i) s += f32x2{blo(pv[i][e]), bhi(pv[i][e])};
                o[e] = san(pk(xg_sum(s.x), xg_sum(s.y)));
            }
            if (lane < 16) stg128(rWS, WS_KVF + head * 4096 + chunk * 256 + lane * 16, o);
        }
        PTA(6);
        _Pragma("unroll 1") for (u32 sp = 0; sp < SPIN_CAP; ++sp) {
            SLEEP(8);
#pragma unroll
            for (int i = 0; i < 8; ++i) pv[i] = ldg128(rWS, WS_KVF + 2 * j * 4096 + lane * 16 + i * 1024);
            if (!has_empty(pv)) break;
        }
#pragma unroll
        for (int i = 0; i < 8; ++i) sts128(MR + i * 1024 + lane * 16, pv[i]);
        sig(L_HELP, 101);
        PTA(7);
        wait_ge(aC, 60);
        if (lane == 0) stg64(rWS, WS_FRS + rt * 128 + j * 8, u32x2{tag, taghi});
        PTA(8);
        _Pragma("unroll 1") for (u32 sp = 0; sp < SPIN_CAP; ++sp) {
            u32x2 fl = ldg64(rWS, WS_FRS + rt * 128 + (lane & 7) * 8);
            if (__all(fl.x == tag && fl.y == taghi)) break;
            asm volatile("" ::: "memory");
        }
        sig(L_HELP, 103);
        PTA(9);
        asm volatile(".Lfk_e1:");
        if (b == 0 && lane == 0) __builtin_amdgcn_raw_buffer_store_b32(tag, rWS, WS_EPOCH, 0, SC1);
    } else {
        const u32 mt = w & 3, hf = w >> 2;
        const u32 myrow = 16 * mt + c;
        DEF_TR();
        const u32 myP = L_PROG + w * 4;
#define SIGP(n) sig(myP, (n))
        const u32 aS = L_PROG + (c < 13 ? c : 0) * 4;
        u32 sv = 0;
#define NEEDL(n, id) do { if (__builtin_expect(!__all(c < 10 || c > 12 || sv >= LT(n)), 0)) { PTA(id); snap_wait(aS, (c >= 10 && c <= 12) ? LT(n) : 0u, sv); PTB(id); } } while (0)
#define NEEDW(wd, n, id) do { if (__builtin_expect(rl(sv, wd) < (u32)(n), 0)) { PTA(id); snap_wait(aS, c == (wd) ? (u32)(n) : 0u, sv); PTB(id); } } while (0)
#define NEEDP(n, id) do { if (__builtin_expect(!__all(c >= 8 || sv >= (u32)(n)), 0)) { PTA(id); snap_wait(aS, c < 8 ? (u32)(n) : 0u, sv); PTB(id); } } while (0)
        u32 hoff, x1o;
        {
            const u32 nt = 2 * hf + (g & 1);
            const u32 grp = (4 * j + nt) / 7;
            const int dlt = grp == 0 ? -1 : (grp == 1 ? 1 : (grp == 2 ? -2 : 2));
            const u32 uu = 2 * nt + (g >> 1);
            const u32 cpart = (((2 * j + (uu >> 2)) * 64 + (uu & 3) * 16) * 16) | psnS;
            const int R = row0 + myrow, Rd = R - dlt;
            hoff = (u32)Rd < (u32)SEQ ? WS_H + cpart + (Rd >> 4) * (14 * 1024) + (Rd & 15) * 16 : 0xfffffff0u;
            const u32 own = cpart + (R >> 4) * (14 * 1024) + (R & 15) * 16;
            x1o = WS_X1 + own;
            SLEEP(18);
            stg128(rWS, hoff, EMPTY);
            stg128(rWS, x1o, EMPTY);
            if (rt == 0 || rt == 31)
                stg128(rWS, (u32)(R + dlt) < (u32)SEQ ? 0xfffffff0u : WS_H + own, u32x4{0, 0, 0, 0});
        }
        NEEDL(1, 1);
        u32x4 xf[14];
        u32x2 xres[2];
        u32x4 wpf[2];
        {
            const u32 sx = (myrow >> 1) & 7;
            const u32 a0 = myrow * 128 + ((g ^ sx) * 16), a1 = a0 ^ 64;
#pragma unroll
            for (int ks = 0; ks < 14; ++ks) xf[ks] = lds128((ks >> 1) * XT_CB + ((ks & 1) ? a1 : a0));
#pragma unroll
            for (int t = 0; t < 2; ++t) xres[t] = lds64((hasB ? j : 0) * XT_CB + myrow * 128 + (((2 * (2 * hf + t) + (g >> 1)) ^ sx) * 16) + (g & 1) * 8);
            SIGP(1);
        }
        {
            f32x4 s0 = {};
            if (hf == 0) {
#pragma unroll
                for (int ks = 0; ks < 14; ++ks) s0 = mfma(ONES, xf[ks], s0);
                if (g == 0) stsf(L_SUM + myrow * 4, s0[0]);
            } else {
#pragma unroll
                for (int ks = 0; ks < 14; ++ks) s0 = mfma(xf[ks], xf[ks], s0);
                if (g == (c >> 2)) stsf(L_SQ + myrow * 4, diag4(s0, c));
            }
        }
        const u32 ta0 = TRA(2 * hf), ta1 = TRA(2 * hf + 1);
        {
            f32x4 a1c[2] = {};
            NEEDL(2, 2);
            f32x4 cs4[2] = {};
            proj7<0, true>(E2 + ta0, E2 + ta1, xf, a1c, aS, sv, mt, cs4);
            WAITV(0);
            SIGP(2);
            NEEDL(3, 3);
            proj7<7, true>(E3 + ta0, E3 + ta1, xf, a1c, aS, sv, mt, cs4);
            if (c == 0) { sts128(L_CS1 + mt * 256 + (32 * hf + 4 * g) * 4, __builtin_bit_cast(u32x4, cs4[0])); sts128(L_CS1 + mt * 256 + (32 * hf + 16 + 4 * g) * 4, __builtin_bit_cast(u32x4, cs4[1])); }
            SIGP(3);
            NEEDP(3, 4);
            const float mean = ldsf(L_SUM + myrow * 4) * (1.f / DM);
            const float var = fmaxf(ldsf(L_SQ + myrow * 4) * (1.f / DM) - mean * mean, 0.f);
            const float rstd = __builtin_amdgcn_rsqf(var + 1e-5f);
            const float k1 = -GC1 * rstd, k2 = -GC2 * rstd * rstd * rstd;
            u32 pkd[2][2];
#pragma unroll
            for (int t = 0; t < 2; ++t) {
                const u32 ca = L_CS1 + (16 * (2 * hf + t) + 4 * g) * 4;
                const f32x4 cs = (__builtin_bit_cast(f32x4, lds128(ca)) + __builtin_bit_cast(f32x4, lds128(ca + 256))) + (__builtin_bit_cast(f32x4, lds128(ca + 512)) + __builtin_bit_cast(f32x4, lds128(ca + 768)));
                f32x2 y0 = f32x2{a1c[t][0], a1c[t][1]} - mean * f32x2{cs[0], cs[1]}, y1 = f32x2{a1c[t][2], a1c[t][3]} - mean * f32x2{cs[2], cs[3]};
                pkd[t][0] = san(pk2(gelu2(y0, k1, k2, rstd))); pkd[t][1] = san(pk2(gelu2(y1, k1, k2, rstd)));
            }
            stg128(rWS, hoff, swap_units(pkd[0][0], pkd[0][1], pkd[1][0], pkd[1][1]));
        }
        PTA(34);
        f32x4 ak[2] = {}, av[2] = {};
        NEEDL(4, 5);
        proj7<0>(E4 + ta0, E4 + ta1, xf, ak, aS, sv);
        NEEDW(8, 1, 30);
#pragma unroll
        for (int ft = 0; ft < 2; ++ft) {
            u32 a = WPHI + hf * 1568 + (4 * g + q4) * 56 + ft * 32 + pp * 8;
            u32x4 t = ldw2(a, a + 16 * 56);
            if (g == 3) { t.z = 0; t.w = 0; }
            wpf[ft] = t;
        }
        if (hf == 0) {
            f32x4 s0 = {};
#pragma unroll
            for (int ks = 0; ks < 14; ++ks) s0 = mfma(lds128(GWL + ks * 64 + g * 16), xf[ks], s0);
            if (g == 0) stsf(L_GATE + myrow * 4, __builtin_amdgcn_rcpf(1.f + __builtin_amdgcn_exp2f(-LOG2E * s0[0])));
        }
        SIGP(4);
        {
            f32x4 a2[4] = {};
            u32x4 hfr[7];
            const u32 goff = (WS_H + rt * 57344 + ((mt * 14 + 7 * hf) * 64 + lane) * 16) | psnS;
            NEEDW(8, 100, 7);
#pragma unroll
            for (int i = 0; i < 7; ++i) hfr[i] = ldg128(rWS, goff + i * 1024);
            NEEDL(7, 6);
            proj7<0>(E2 + ta0, E2 + ta1, xf, av, aS, sv);
            _Pragma("unroll 1") for (u32 sp = 0; sp < SPIN_CAP && has_empty(hfr); ++sp) {
                SLEEP(6);
#pragma unroll
                for (int i = 0; i < 7; ++i) hfr[i] = ldg128(rWS, goff + i * 1024);
            }
            PTA(35);
            const u32 wb = hf ? E1 : E0;
            u32 t0 = wb + TRA(0), t1 = wb + TRA(1), t2 = wb + TRA(2), t3 = wb + TRA(3);
            PIN(t0); PIN(t1); PIN(t2); PIN(t3);
            u32x4 f[2][4];
            f[0][0] = ldw(t0); f[0][1] = ldw(t1); f[0][2] = ldw(t2); f[0][3] = ldw(t3);
            SB();
#pragma unroll
            for (int i = 0; i < 7; ++i) {
                if (i < 6) { f[(i + 1) & 1][0] = ldw(t0 + (i + 1) * 4096); f[(i + 1) & 1][1] = ldw(t1 + (i + 1) * 4096); }
                a2[0] = mfma(f[i & 1][0], hfr[i], a2[0]); a2[1] = mfma(f[i & 1][1], hfr[i], a2[1]);
                PIN(a2[0]); PIN(a2[1]);
                SB();
                if (i < 6) { f[(i + 1) & 1][2] = ldw(t2 + (i + 1) * 4096); f[(i + 1) & 1][3] = ldw(t3 + (i + 1) * 4096); }
                a2[2] = mfma(f[i & 1][2], hfr[i], a2[2]); a2[3] = mfma(f[i & 1][3], hfr[i], a2[3]);
                PIN(a2[2]); PIN(a2[3]);
                SB();
            }
            NEEDP(4, 33);
            const u32 sc = (hf ? MR : XX) + mt * 2048 + lane * 16;
            sts128(sc, __builtin_bit_cast(u32x4, hf ? a2[0] : a2[2]));
            sts128(sc + 1024, __builtin_bit_cast(u32x4, hf ? a2[1] : a2[3]));
            SIGP(5);
            PTA(9); wait_ge(L_PROG + (w ^ 4) * 4, 5); PTB(9);
            const u32 sr = (hf ? XX : MR) + mt * 2048 + lane * 16;
            u32 pkd[2][2];
#pragma unroll
            for (int t = 0; t < 2; ++t) {
                f32x4 o = __builtin_bit_cast(f32x4, lds128(sr + t * 1024));
                f32x4 s = (hf ? a2[2 + t] : a2[t]) + o;
                pkd[t][0] = san(pk(s[0] + blo(xres[t].x), s[1] + bhi(xres[t].x)));
                pkd[t][1] = san(pk(s[2] + blo(xres[t].y), s[3] + bhi(xres[t].y)));
            }
            stg128(rWS, x1o, swap_units(pkd[0][0], pkd[0][1], pkd[1][0], pkd[1][1]));
        }
        u32 ln1 = lane; PIN(ln1);
        const u32 wsw = ((ln1 >> 1) & 1) | (((ln1 >> 3) & 1) << 1);
        const u32 wrb = E2 + (16 * mt + (ln1 & 15)) * 128 + (ln1 >> 4) * 8;
        PTA(36);
        NEEDL(8, 10);
        proj7<7>(E3 + ta0, E3 + ta1, xf, ak, aS, sv);
        SIGP(7);
        {
            u32x4 pos, neg;
            features(ak, wpf, g, pos, neg);
            NEEDP(5, 32);
            *(LAS u32x2*)(wrb + hf * 8192 + ((0 ^ wsw) << 5)) = u32x2{pos.x, pos.y};
            *(LAS u32x2*)(wrb + hf * 8192 + ((1 ^ wsw) << 5)) = u32x2{pos.z, pos.w};
            *(LAS u32x2*)(wrb + hf * 8192 + ((2 ^ wsw) << 5)) = u32x2{neg.x, neg.y};
            *(LAS u32x2*)(wrb + hf * 8192 + ((3 ^ wsw) << 5)) = u32x2{neg.z, neg.w};
        }
        NEEDL(9, 11);
        proj7<7>(E4 + ta0, E4 + ta1, xf, av, aS, sv);
        {
            u32x4 vv = pk8(av[0], av[1]);
            if (g == 3) { vv.z = 0x00003f80u; vv.w = 0; }
            *(LAS u32x2*)(wrb + 16384 + (((2 * hf) ^ wsw) << 5)) = u32x2{vv.x, vv.y};
            *(LAS u32x2*)(wrb + 16384 + (((2 * hf + 1) ^ wsw) << 5)) = u32x2{vv.z, vv.w};
        }
        SIGP(8);
        PTA(37);
        u32x4 phq[2];
        {
            f32x4 aq[2] = {};
            NEEDL(10, 12);
            proj7<0>(E0 + ta0, E0 + ta1, xf, aq, aS, sv);
            NEEDP(8, 13);
            {
                f32x4 kv[2] = {};
                const u32 pa = E2 + hf * 8192 + TRA(2 * (mt >> 1)), pb = E2 + hf * 8192 + TRA(2 * (mt >> 1) + 1), va = E2 + 16384 + TRA(2 * hf + (mt & 1));
#pragma unroll
                for (int ks = 0; ks < 2; ++ks) {
                    const u32x4 vf = ldw(va + ks * 4096);
                    kv[0] = mfma(ldw(pa + ks * 4096), vf, kv[0]);
                    kv[1] = mfma(ldw(pb + ks * 4096), vf, kv[1]);
                }
                SIGP(9);
                u32x4 kp = pk8(kv[0], kv[1]);
                kp = u32x4{san(kp.x), san(kp.y), san(kp.z), san(kp.w)};
                NEEDW(9, 60, 38);
                stg128(rWS, WS_KVP + ((2 * j + hf) * 32 + rt) * 4096 + mt * 1024 + lane * 16, kp);
            }
            u32x4 xv[7];
            const u32 x1off = WS_X1 + rt * 57344 + ((mt * 14 + 7 * hf) * 64 + lane) * 16;
#pragma unroll
            for (int i = 0; i < 7; ++i) xv[i] = ldg128(rWS, x1off + i * 1024);
            NEEDL(11, 14);
            proj7<7>(E1 + ta0, E1 + ta1, xf, aq, aS, sv);
            SIGP(10);
            features(aq, wpf, g, phq[0], phq[1]);
            SB();
            _Pragma("unroll 1") for (u32 sp = 0; sp < SPIN_CAP && has_empty(xv); ++sp) {
                SLEEP(4);
#pragma unroll
                for (int i = 0; i < 7; ++i) xv[i] = ldg128(rWS, x1off + i * 1024);
            }
            NEEDP(10, 15);
            const u32 dst = ((7 * hf * 4 + mt) * 64 + lane) * 16;
#pragma unroll
            for (int i = 0; i < 7; ++i) sts128(dst + i * 4096, xv[i]);
            SIGP(11);
        }
        u32 ln = lane; PIN(ln);
        const u32 g2 = ln >> 4, c2 = ln & 15;
        const u32 trs2 = ((ln >> 3) & 1) | ((g2 & 1) << 1);
        const u32 trb2 = (8 * g2 + (c2 >> 2)) * 128 + (ln & 3) * 8;
#define TRB(t) (trb2 + (((t) ^ trs2) << 5))
        const u32 aC2 = L_PROG + (ln & 7) * 4, aL2 = L_LAND + (c2 < 3 ? c2 : 0) * 4;
        const u32 mh4 = w & 1, nt4 = w >> 1;
        u32 rsoff;
        {
            const u32 row = 16 * (2 * mh4 + (g2 & 1)) + c2;
            rsoff = WS_RS + ((rt * 8 + (row >> 3)) * 8 + j) * 7168 + (nt4 * 8 + (row & 7)) * 32 + (g2 >> 1) * 16;
        }
        constexpr u32 G_OFF[14] = {86016, 98304, 110592, 122880, 61440, 73728, 86016, 98304, 110592, 0, 122880, 61440, 73728, 12288};
        constexpr u32 S_OFF[7] = {135168, 139264, 143360, 147456, 57344, 135168, 139264};
        constexpr u32 G_LAND[14] = {87, 91, 94, 98, 102, 106, 110, 113, 117, 120, 123, 127, 130, 133};
        constexpr u32 S_LAND[7] = {88, 95, 99, 103, 107, 114, 124};
        constexpr u32 H_OFF[21] = {86016, 98304, 110592, 24576, 0, 122880, 61440, 36864, 73728, 12288, 49152, 135168, 86016, 98304, 110592, 24576, 0, 122880, 61440, 36864, 73728};
        constexpr u32 B_LAND[7] = {142, 151, 160, 169, 178, 187, 196};
        constexpr u32 AF_OFF[3] = {12288, 49152, 135168};

        const u32 xb = ln * 16;
#define SKEW 2
        if (w < 7) {
            f32x4 a3[4][2], a3s[2];
            u32x4 f[2][2], xr[2][4], on3 = ONES;
            u32 pa = ln == 0 ? myP : (ln == 1 ? myP + 64 : MISC + 1536 + ln * 4), two = ln == 1 ? 8 : 2;
            const u32 aQ = L_PROG + (c2 < 7 ? 16 + c2 : (c2 < 10 ? c2 + 3 : 10)) * 4;
            PIN(on3); PIN(pa); PIN(two);
#define MFMA_ACC(acc, a, b) asm volatile("v_mfma_f32_16x16x32_bf16 %0, %1, %2, %0" : "+v"(acc) : "v"(a), "v"(b))
#define MFMA_K(ks, acc, a, b) do { if ((ks) == 0) asm volatile("v_mfma_f32_16x16x32_bf16 %0, %1, %2, 0" : "=&v"(acc) : "v"(a), "v"(b)); else MFMA_ACC(acc, a, b); } while (0)
#define S3_RX(ks, m) xr[(ks) & 1][m] = lds128(xb + ((ks) * 4 + (m)) * 1024)
#define S3_LOOP(RW, LAND) do { \
            PTA(16); wait_ge(aC2, 11); wait_ge(aL2, G_LAND[1]); PTB(16); \
            u32 lv = 0; \
            RW(0, 0); RW(0, 1); S3_RX(0, 0); S3_RX(0, 1); S3_RX(0, 2); S3_RX(0, 3); \
            SB(); \
            _Pragma("unroll") for (int ks = 0; ks < 14; ++ks) { \
                const int kn = ks < 13 ? ks + 1 : 13; \
                if ((ks & 1) && ks < 13) { if (__builtin_expect(!__all(lv >= (u32)(98 + 4 * (ks - 1))), 0)) { PTA(39); wait_ge(aQ, 98 + 4 * (ks - 1)); PTB(39); } } \
                MFMA_K(ks, a3s[0], f[ks & 1][0], on3); \
                if (ks < 13) RW(kn, 0); \
                SB(); \
                MFMA_K(ks, a3s[1], f[ks & 1][1], on3); \
                if (ks < 13) RW(kn, 1); \
                SB(); \
                _Pragma("unroll") for (int m = 0; m < 4; ++m) { \
                    MFMA_K(ks, a3[m][0], f[ks & 1][0], xr[ks & 1][m]); \
                    if (ks < 13) S3_RX(kn, m); \
                    SB(); \
                    MFMA_K(ks, a3[m][1], f[ks & 1][1], xr[ks & 1][m]); \
                    if (m == 1 && !(ks & 1)) lv = peek(aQ); \
                    if (m == 3 && !(ks & 1)) asm volatile("ds_add_u32 %0, %1" ::"v"(pa), "v"(two) : "memory"); \
                    SB(); \
                } \
            } } while (0)
            if (w < 6) {
                u32 wb[2][3];
#pragma unroll
                for (int t = 0; t < 2; ++t)
#pragma unroll
                    for (int q = 0; q < 3; ++q) { wb[t][q] = (w >> 1) * 4096 + TRB(2 * (w & 1) + t) + q * 49152; PIN(wb[t][q]); }
#define S3_RWG(ks, t) f[(ks) & 1][t] = ldw(wb[t][G_OFF[ks] / 49152] + G_OFF[ks] % 49152)
#define S3_LG(ks) G_LAND[ks]
                S3_LOOP(S3_RWG, S3_LG);
            } else {
                u32 wl[4], wh[4];
#pragma unroll
                for (int t = 0; t < 4; ++t) { wl[t] = TRB(t); wh[t] = wl[t] + 33 * 4096; PIN(wl[t]); PIN(wh[t]); }
#define S3_RWS(ks, t) f[(ks) & 1][t] = ldw(S_OFF[(ks) >> 1] < 65536 ? wl[2 * ((ks) & 1) + (t)] + S_OFF[(ks) >> 1] : wh[2 * ((ks) & 1) + (t)] + (S_OFF[(ks) >> 1] - 33 * 4096))
#define S3_LS(ks) S_LAND[(ks) >> 1]
                S3_LOOP(S3_RWS, S3_LS);
            }
            asm volatile("s_nop 7" ::: "memory");
            SIGP(26);
            PTA(17); wait_ge(aC2, 26); PTB(17);
            float mean[4], k1[4], k2[4], so[4];
#pragma unroll
            for (int m = 0; m < 4; ++m) {
                const u32 row = 16 * m + c2;
                mean[m] = ldsf(L_SUM1 + row * 4) * (1.f / DM);
                const float var = fmaxf(ldsf(L_SQ1 + row * 4) * (1.f / DM) - mean[m] * mean[m], 0.f);
                const float rstd = __builtin_amdgcn_rsqf(var + 1e-5f);
                k1[m] = -GC1 * rstd; k2[m] = -GC2 * rstd * rstd * rstd; so[m] = rstd * (1.f - ldsf(L_GATE + row * 4));
            }
            const u32 hdst = (w < 3 ? AF_OFF[0] : (w < 6 ? AF_OFF[1] : AF_OFF[2])) + (w % 3) * 4096 + ln * 16;
#pragma unroll
            for (int m = 0; m < 4; ++m) {
                u32x4 o;
#pragma unroll
                for (int t = 0; t < 2; ++t) {
                    f32x2 y0 = f32x2{a3[m][t][0], a3[m][t][1]} - mean[m] * f32x2{a3s[t][0], a3s[t][1]};
                    f32x2 y1 = f32x2{a3[m][t][2], a3[m][t][3]} - mean[m] * f32x2{a3s[t][2], a3s[t][3]};
                    o[2 * t] = pk2(gelu2(y0, k1[m], k2[m], so[m])); o[2 * t + 1] = pk2(gelu2(y1, k1[m], k2[m], so[m]));
                }
                sts128(hdst + m * 1024, o);
            }
        } else {
            f32x4 s1[4] = {}, s2[4] = {};
            u32x4 v[2][8];
            wait_ge(aC2, 11);
#pragma unroll
            for (int q = 0; q < 8; ++q) v[0][q] = lds128(xb + q * 1024);
            SIGP(13);
            SB();
#pragma unroll
            for (int i = 0; i < 7; ++i) {
                if (i < 6) {
#pragma unroll
                    for (int q = 0; q < 8; ++q) v[(i + 1) & 1][q] = lds128(xb + ((i + 1) * 8 + q) * 1024);
                    SIGP(15 + 2 * i);
                }
                SB();
#pragma unroll
                for (int q = 0; q < 8; ++q) { s1[q & 3] = mfma(ONES, v[i & 1][q], s1[q & 3]); s2[q & 3] = mfma(v[i & 1][q], v[i & 1][q], s2[q & 3]); }
#pragma unroll
                for (int m = 0; m < 4; ++m) { PIN(s1[m]); PIN(s2[m]); }
                SB();
            }
            if (g2 == (c2 >> 2)) {
#pragma unroll
                for (int m = 0; m < 4; ++m) { stsf(L_SUM1 + (16 * m + c2) * 4, s1[m][0]); stsf(L_SQ1 + (16 * m + c2) * 4, diag4(s2[m], c2)); }
            }
            SIGP(26);
            wait_ge(aC2, 26);
        }
        u32x4 af[2][9];
        {
            PTA(18); wait_ge(L_HELP, 101); PTB(18);
            u32x4 kf[4];
#pragma unroll
            for (int i = 0; i < 4; ++i) kf[i] = lds128(MR + (hf * 4 + i) * 1024 + ln * 16);
            const float gt = ldsf(L_GATE + (16 * mt + c2) * 4);
            f32x4 o0 = mfma(kf[0], phq[0], f32x4{}); o0 = mfma(kf[2], phq[1], o0);
            f32x4 o1 = mfma(kf[1], phq[0], f32x4{}); o1 = mfma(kf[3], phq[1], o1);
            const float den = xg_sum(g2 == 3 ? o1[0] : 0.f) + 1e-6f;
            const float sc = gt * __builtin_amdgcn_rcpf(den);
            u32x4 f = pk8(o0 * sc, o1 * sc);
            if (g2 == 3) { f.z = 0; f.w = 0; }
            sts128(AF_OFF[2] + (1 + hf) * 4096 + mt * 1024 + ln * 16, f);
            SIGP(27);
        }
        {
            u32 wb4[3];
            wb4[0] = TRB(nt4); wb4[1] = wb4[0] + 61440; wb4[2] = wb4[0] + 122880;
            PIN(wb4[0]); PIN(wb4[1]); PIN(wb4[2]);
#define W4_OFF(cb, k) (H_OFF[3 * (cb) + (k) / 3] + ((k) % 3) * 4096)
#define W4_RD(cb, k) ldw(wb4[W4_OFF(cb, k) / 61440] + W4_OFF(cb, k) % 61440)
            u32x4 f[2][7];
            wait_ge(aL2, B_LAND[0]);
#pragma unroll
            for (int k = 0; k < 7; ++k) f[0][k] = W4_RD(0, k);
            PTA(19); wait_ge(aC2, 27); PTB(19);
            {
                u32 ab = (2 * mh4) * 1024 + ln * 16, ab2 = ab + AF_OFF[2];
                PIN(ab); PIN(ab2);
#pragma unroll
                for (int k = 0; k < 9; ++k)
#pragma unroll
                    for (int m = 0; m < 2; ++m) af[m][k] = lds128((k < 6 ? ab + AF_OFF[k / 3] : ab2) + (k % 3) * 4096 + m * 1024);
            }
            SIGP(30);
            u32 lv = peek(aL2);
#pragma unroll
            for (int cb = 1; cb < 8; ++cb) {
                const int p = cb - 1;
                if (cb < 7) { if (__builtin_expect(!__all(lv >= B_LAND[cb < 7 ? cb : 0]), 0)) { PTA(20 + cb); wait_ge(aL2, B_LAND[cb < 7 ? cb : 0]); PTB(20 + cb); } }
                const u32x4 wo0 = W4_RD(p, 7), wo1 = W4_RD(p, 8);
                f32x4 a4[2];
#pragma unroll
                for (int k = 0; k < 7; ++k) {
                    if (k == 0) asm volatile("v_mfma_f32_16x16x32_bf16 %0, %1, %2, 0" : "=&v"(a4[0]) : "v"(f[p & 1][0]), "v"(af[0][0]));
                    else MFMA_ACC(a4[0], f[p & 1][k], af[0][k]);
                    if (cb < 7) f[cb & 1][k] = W4_RD(cb < 7 ? cb : 0, k);
                    SB();
                    if (k == 0) asm volatile("v_mfma_f32_16x16x32_bf16 %0, %1, %2, 0" : "=&v"(a4[1]) : "v"(f[p & 1][0]), "v"(af[1][0]));
                    else MFMA_ACC(a4[1], f[p & 1][k], af[1][k]);
                    if (k == 3) lv = peek(aL2);
                    if (cb <= 3 && k == 5) SIGP(29 + 3 * cb + 1);
                    SB();
                }
                MFMA_ACC(a4[0], wo0, af[0][7]); MFMA_ACC(a4[1], wo0, af[1][7]);
                MFMA_ACC(a4[0], wo1, af[0][8]); MFMA_ACC(a4[1], wo1, af[1][8]);
                asm volatile("s_nop 7" : "+v"(a4[0]), "+v"(a4[1]));
                u32x4 u = swap_units(pk(a4[0][0], a4[0][1]), pk(a4[0][2], a4[0][3]), pk(a4[1][0], a4[1][1]), pk(a4[1][2], a4[1][3]));
                stg128(rWS, rsoff + p * 1024, u);
                SB();
            }
        }
        PTA(27);
        WAITV(0);
        SIGP(60);
        PTA(28);
        wait_ge(L_HELP, 103);
        PTA(29);
        asm volatile(".Lfk_e2:");
        if (w < 7) finish_block(w);
    }
}
}

extern "C" void kernel_launch(void* const* d_in, const int* in_sizes, int n_in, void* d_out, int out_size, void* d_ws, size_t ws_size,
                              hipStream_t stream) {
    (void)hipFuncSetAttribute((const void*)fused_kernel, hipFuncAttributeMaxDynamicSharedMemorySize, LDS_BYTES);
    fused_kernel<<<256, NWAVE * 64, LDS_BYTES, stream>>>((const u16*)d_in[0], (const u16*)d_in[1], (const u16*)d_in[2], (const u16*)d_in[3],
                                                         (const u16*)d_in[5], (const u16*)d_in[6], (const u16*)d_in[7], (const u16*)d_in[8],
                                                         (const u16*)d_in[9], (u16*)d_out, (unsigned char*)d_ws);
}
```

```cpp
#include <hip/hip_runtime.h>
#include <hip/hip_bf16.h>

namespace {
typedef unsigned short u16;
typedef unsigned u32;
typedef unsigned long long u64;
typedef short bf16x8 __attribute__((ext_vector_type(8)));
typedef short s16x4 __attribute__((ext_vector_type(4)));
typedef float f32x4 __attribute__((ext_vector_type(4)));
typedef float f32x2 __attribute__((ext_vector_type(2)));
typedef __bf16 bf16x2 __attribute__((ext_vector_type(2)));
typedef u32 u32x4 __attribute__((ext_vector_type(4)));
typedef u32 u32x2 __attribute__((ext_vector_type(2)));
#define LAS __attribute__((address_space(3)))
#define GAS __attribute__((address_space(1)))
#define DI __device__ __forceinline__
typedef __amdgpu_buffer_rsrc_t rsrc_t;

constexpr int SEQ = 2048, DM = 448, FF = 1792;

constexpr u32 E0 = 0, E1 = 28672, E2 = 57344, E3 = 86016, E4 = 114688, XX = 143360, MR = 151552, MISC = 159744;
constexpr u32 LDS_BYTES = 163840;
constexpr u32 L_SUM = MISC, L_SQ = MISC + 256, L_GATE = MISC + 512, L_SUM1 = MISC + 768, L_SQ1 = MISC + 1024, L_CS1 = MISC + 3072, GWL = MISC + 2048;
constexpr u32 WPHI = MR;
constexpr u32 XT_CB = 8192;

constexpr u32 WS_EPOCH = 0;
constexpr u32 WS_FCLR = 4096;
constexpr u32 WS_FKV = 12288;
constexpr u32 WS_FRS = 16384;
constexpr u32 WS_H = 65536;
constexpr u32 WS_X1 = WS_H + 32 * 57344;
constexpr u32 WS_KVP = WS_X1 + 32 * 57344;
constexpr u32 WS_KVF = WS_KVP + 16 * 32 * 8192;
constexpr u32 WS_RS = WS_KVF + 16 * 4096;
constexpr u32 WS_MB1 = WS_RS + 32 * 64 * 7168;
constexpr u32 WS_MB2 = WS_MB1 + 131072;
constexpr u32 WS_MB3 = WS_MB2 + 131072;
constexpr u32 WS_END = WS_MB3 + 131072;
constexpr u32 SPIN_CAP = 1u << 16;
constexpr int SC1 = 16;
constexpr u32 CODE_BYTES = 36864;

DI f32x4 mfma(u32x4 a, u32x4 b, f32x4 c) {
    return __builtin_amdgcn_mfma_f32_16x16x32_bf16(__builtin_bit_cast(bf16x8, a), __builtin_bit_cast(bf16x8, b), c, 0, 0, 0);
}
DI u32 pk(float a, float b) { f32x2 f = {a, b}; return __builtin_bit_cast(u32, __builtin_convertvector(f, bf16x2)); }
DI u32 pk2(f32x2 f) { return __builtin_bit_cast(u32, __builtin_convertvector(f, bf16x2)); }
DI u32x4 pk8(f32x4 a, f32x4 b) { return u32x4{pk(a[0], a[1]), pk(a[2], a[3]), pk(b[0], b[1]), pk(b[2], b[3])}; }
DI float blo(u32 v) { return __builtin_bit_cast(float, v << 16); }
DI float bhi(u32 v) { return __builtin_bit_cast(float, v & 0xffff0000u); }
DI u32x4 lds128(u32 a) { return *(LAS const u32x4*)a; }
DI u32x2 lds64(u32 a) { return *(LAS const u32x2*)a; }
DI float ldsf(u32 a) { return *(LAS const float*)a; }
DI void sts128(u32 a, u32x4 v) { *(LAS u32x4*)a = v; }
DI void stsf(u32 a, float v) { *(LAS float*)a = v; }
DI u32x4 ldw2(u32 a0, u32 a1) {
    s16x4 lo = __builtin_amdgcn_ds_read_tr16_b64_v4i16((LAS s16x4*)a0);
    s16x4 hi = __builtin_amdgcn_ds_read_tr16_b64_v4i16((LAS s16x4*)a1);
    u32x2 l = __builtin_bit_cast(u32x2, lo), h = __builtin_bit_cast(u32x2, hi);
    return u32x4{l.x, l.y, h.x, h.y};
}
DI u32x4 ldw(u32 a) { return ldw2(a, a + 512); }
DI void dma16(rsrc_t rs, u32 voff, u32 soff, u32 lds) {
    asm volatile("s_mov_b32 m0, %0\n\ts_nop 0\n\tbuffer_load_dwordx4 %1, %2, %3 offen lds" ::"s"(lds), "v"(voff), "s"(rs), "s"(soff) : "memory");
}
DI void dma_pair(rsrc_t rs, u32 v0, u32 v1, u32 soff, u32 lds) {
    asm volatile("s_mov_b32 m0, %0\n\ts_nop 0\n\t"
                 "buffer_load_dwordx4 %1, %3, %4 offen lds\n\t"
                 "buffer_load_dwordx4 %2, %3, %4 offen offset:1024 lds"
                 ::"s"(lds), "v"(v0), "v"(v1), "s"(rs), "s"(soff) : "memory");
}
struct Pat { u32 v[4]; };
DI Pat mkpat(u32 a, u32 b, u32 step) { Pat p; p.v[0] = a; p.v[1] = b + (step - 1024); p.v[2] = a + 2 * (step - 1024); p.v[3] = b + 3 * (step - 1024); return p; }
DI void dma_unit(rsrc_t rs, const Pat& p, u32 soff, u32 lds) {
    asm volatile("s_mov_b32 m0, %0\n\ts_nop 0\n\t"
                 "buffer_load_dwordx4 %1, %5, %6 offen lds\n\t"
                 "buffer_load_dwordx4 %2, %5, %6 offen offset:1024 lds\n\t"
                 "buffer_load_dwordx4 %3, %5, %6 offen offset:2048 lds\n\t"
                 "buffer_load_dwordx4 %4, %5, %6 offen offset:3072 lds"
                 ::"s"(lds), "v"(p.v[0]), "v"(p.v[1]), "v"(p.v[2]), "v"(p.v[3]), "s"(rs), "s"(soff) : "memory");
}
#define WAITV(n) asm volatile("s_waitcnt vmcnt(" #n ")" ::: "memory")
#define SLEEP(n) asm volatile("s_sleep " #n ::: "memory")
#define BAR() asm volatile("s_waitcnt lgkmcnt(0)\n\ts_barrier" ::: "memory")
#define SB() __builtin_amdgcn_sched_barrier(0)
DI u32x4 ldg128(rsrc_t rs, u32 off) { return __builtin_amdgcn_raw_buffer_load_b128(rs, off, 0, SC1); }
DI u32x2 ldg64(rsrc_t rs, u32 off) { return __builtin_amdgcn_raw_buffer_load_b64(rs, off, 0, SC1); }
DI void stg128(rsrc_t rs, u32 off, u32x4 v) { __builtin_amdgcn_raw_buffer_store_b128(v, rs, off, 0, SC1); }
DI void stg64(rsrc_t rs, u32 off, u32x2 v) { __builtin_amdgcn_raw_buffer_store_b64(v, rs, off, 0, SC1); }
DI u32 san(u32 v) { return v < 0xfffffffeu ? v : 0xfffffffeu; }
DI u32 umax3(u32 a, u32 b, u32 c) { u32 m = a > b ? a : b; return m > c ? m : c; }
DI u32 fmax4(u32 m, u32x4 v) { return umax3(umax3(m, v.x, v.y), v.z, v.w); }
template <int N> DI bool has_empty(const u32x4 (&f)[N]) {
    u32 m = 0;
#pragma unroll
    for (int i = 0; i < N; ++i) m = fmax4(m, f[i]);
    return __any(m == 0xffffffffu);
}
DI f32x2 gelu2(f32x2 y, float k1, float k2, float so) {
    f32x2 t = __builtin_elementwise_fma(y * y, f32x2{k2, k2}, f32x2{k1, k1}) * y;
    f32x2 d = f32x2{__builtin_amdgcn_exp2f(t.x), __builtin_amdgcn_exp2f(t.y)} + 1.f;
    f32x2 r = {__builtin_amdgcn_rcpf(d.x), __builtin_amdgcn_rcpf(d.y)};
    return (y * so) * r;
}
constexpr float GC1 = 2.3022082f, GC2 = 0.10294324f;
DI float f_of(u32 v) { return __builtin_bit_cast(float, v); }
DI u32 u_of(float v) { return __builtin_bit_cast(u32, v); }
DI float xg_sum(float v) {
    auto r = __builtin_amdgcn_permlane32_swap(u_of(v), u_of(v), false, false);
    v = f_of(r[0]) + f_of(r[1]);
    auto s = __builtin_amdgcn_permlane16_swap(u_of(v), u_of(v), false, false);
    return f_of(s[0]) + f_of(s[1]);
}
DI float xg_max(float v) {
    auto r = __builtin_amdgcn_permlane32_swap(u_of(v), u_of(v), false, false);
    v = fmaxf(f_of(r[0]), f_of(r[1]));
    auto s = __builtin_amdgcn_permlane16_swap(u_of(v), u_of(v), false, false);
    return fmaxf(f_of(s[0]), f_of(s[1]));
}
template <int CTRL> DI float dpp(float v) {
    return __builtin_bit_cast(float, __builtin_amdgcn_update_dpp(0, __builtin_bit_cast(int, v), CTRL, 0xf, 0xf, true));
}
DI float row_sum(float v) { v += dpp<0x128>(v); v += dpp<0x124>(v); v += dpp<0x122>(v); v += dpp<0x121>(v); return v; }
DI float row_max(float v) {
    asm volatile("s_nop 1\n\tv_max_f32_dpp %0, %0, %0 row_ror:8 row_mask:0xf bank_mask:0xf\n\ts_nop 1\n\t"
                 "v_max_f32_dpp %0, %0, %0 row_ror:4 row_mask:0xf bank_mask:0xf\n\ts_nop 1\n\t"
                 "v_max_f32_dpp %0, %0, %0 row_ror:2 row_mask:0xf bank_mask:0xf\n\ts_nop 1\n\t"
                 "v_max_f32_dpp %0, %0, %0 row_ror:1 row_mask:0xf bank_mask:0xf" : "+v"(v));
    return v;
}
DI u32x4 swap_units(u32 a0, u32 a1, u32 b0, u32 b1) {
    auto r0 = __builtin_amdgcn_permlane16_swap(a0, b0, false, false);
    auto r1 = __builtin_amdgcn_permlane16_swap(a1, b1, false, false);
    return u32x4{(u32)r0[0], (u32)r1[0], (u32)r0[1], (u32)r1[1]};
}
constexpr float LOG2E = 1.4426950408889634f;
DI float diag4(f32x4 d, u32 c) { float a = (c & 1) ? d[1] : d[0], b = (c & 1) ? d[3] : d[2]; return (c & 2) ? b : a; }

#define PTA(id)
#define PTB(id)

#define PIN(x) asm volatile("" : "+v"(x))
constexpr int NWAVE = 12;
constexpr u32 L_PROG = MISC + 1280, L_HELP = L_PROG + 32, L_AUX = L_PROG + 36, L_LAND = L_PROG + 40, L_TAG = L_PROG + 56, L_CNT = L_PROG + 96;
DI void sigv(u32 addr, u32 val) { asm volatile("ds_write_b32 %0, %1" ::"v"(addr), "v"(val) : "memory"); }
DI void sig(u32 addr, u32 val) { asm volatile("s_mov_b64 exec, 1\n\tds_write_b32 %0, %1\n\ts_mov_b64 exec, -1" ::"v"(addr), "v"(val) : "memory"); }
DI u32 peek(u32 addr) { return *(volatile LAS u32*)addr; }
DI void wait_ge(u32 addr, u32 thr) {
    _Pragma("unroll 1") for (u32 sp = 0; sp < (SPIN_CAP << 4); ++sp) {
        u32 v;
        asm volatile("ds_read_b32 %0, %1\n\ts_waitcnt lgkmcnt(0)" : "=v"(v) : "v"(addr) : "memory");
        if (__all(v >= thr)) break;
        SLEEP(1);
    }
}

DI u32 rl(u32 v, int l) { return __builtin_amdgcn_readlane(v, l); }
DI void snap_wait(u32 aS, u32 thr, u32& sv) {
    _Pragma("unroll 1") for (u32 sp = 0; sp < (SPIN_CAP << 4); ++sp) {
        asm volatile("ds_read_b32 %0, %1\n\ts_waitcnt lgkmcnt(0)" : "=v"(sv) : "v"(aS) : "memory");
        if (__all(sv >= thr)) break;
        SLEEP(1);
    }
}
#define MFMA_ACC(acc, a, b) asm volatile("v_mfma_f32_16x16x32_bf16 %0, %1, %2, %0" : "+v"(acc) : "v"(a), "v"(b))
#define MFMA_NEW(acc, a, b) asm volatile("v_mfma_f32_16x16x32_bf16 %0, %1, %2, 0" : "=&v"(acc) : "v"(a), "v"(b))
#define MFMA_K(first, acc, a, b) do { if (first) MFMA_NEW(acc, a, b); else MFMA_ACC(acc, a, b); } while (0)
#define MFMA_DONE2(x, y) asm volatile("s_nop 7" : "+v"(x), "+v"(y))
template <int K0, bool CS = false>
DI void proj7(u32 ent, const u32 (&tr)[4], const u32x4 (&xf)[14], f32x4 (&acc)[4], u32 aS, u32& sv, u32x4 ones = u32x4{}, f32x4* cs = nullptr) {
    u32x4 f[3][4];
    u32 wa[4];
#pragma unroll
    for (int t = 0; t < 4; ++t) { wa[t] = ent + tr[t]; PIN(wa[t]); }
#pragma unroll
    for (int k = 0; k < 2; ++k)
#pragma unroll
        for (int t = 0; t < 4; ++t) f[k][t] = ldw(wa[t] + k * 4096);
    SB();
#pragma unroll
    for (int k = 0; k < 7; ++k) {
#pragma unroll
        for (int t = 0; t < 4; ++t) {
            MFMA_K(K0 + k == 0, acc[t], f[k % 3][t], xf[K0 + k]);
            if (k < 5) f[(k + 2) % 3][t] = ldw(wa[t] + (k + 2) * 4096);
            if (k == 5 && t == 0) sv = peek(aS);
            if (CS) MFMA_K(K0 + k == 0, cs[t], f[k % 3][t], ones);
            SB();
        }
    }
    asm volatile("s_nop 7" : "+v"(acc[0]), "+v"(acc[1]), "+v"(acc[2]), "+v"(acc[3]));
    if (CS) { PIN(cs[0]); PIN(cs[1]); PIN(cs[2]); PIN(cs[3]); }
}

DI void features(f32x4 a0_, f32x4 a1_, const u32x4* wpf, u32 g, u32x4& pos, u32x4& neg) {
    u32x4 qf = pk8(a0_, a1_);
    if (g == 3) { qf.z = 0; qf.w = 0; }
    f32x4 p0 = mfma(wpf[0], qf, f32x4{}) * LOG2E, p1 = mfma(wpf[1], qf, f32x4{}) * LOG2E;
    const bool v1 = g < 3;
    if (!v1) p1 = p0;
    float mx = fmaxf(fmaxf(fmaxf(p0[0], p0[1]), fmaxf(p0[2], p0[3])), fmaxf(fmaxf(p1[0], p1[1]), fmaxf(p1[2], p1[3])));
    float mn = fminf(fminf(fminf(p0[0], p0[1]), fminf(p0[2], p0[3])), fminf(fminf(p1[0], p1[1]), fminf(p1[2], p1[3])));
    mx = xg_max(mx); mn = -xg_max(-mn);
    const f32x4 d0 = p0 - mx, d1 = p1 - mx, e0 = mn - p0, e1 = mn - p1;
    f32x4 a0, a1, b0, b1;
#pragma unroll
    for (int r = 0; r < 4; ++r) {
        a0[r] = __builtin_amdgcn_exp2f(d0[r]); a1[r] = __builtin_amdgcn_exp2f(d1[r]);
        b0[r] = __builtin_amdgcn_exp2f(e0[r]); b1[r] = __builtin_amdgcn_exp2f(e1[r]);
    }
    if (!v1) { a1 = f32x4{}; b1 = f32x4{}; }
    const f32x4 sa4 = a0 + a1, sb4 = b0 + b1;
    const float ra = __builtin_amdgcn_rcpf(xg_sum((sa4[0] + sa4[1]) + (sa4[2] + sa4[3])));
    const float rb = __builtin_amdgcn_rcpf(xg_sum((sb4[0] + sb4[1]) + (sb4[2] + sb4[3])));
    pos = pk8(a0 * ra, a1 * ra);
    neg = pk8(b0 * rb, b1 * rb);
}

__global__ void __launch_bounds__(NWAVE * 64, 3)
fused_kernel(const u16* __restrict__ x, const u16* __restrict__ wqkv, const u16* __restrict__ wphi, const u16* __restrict__ wo,
             const u16* __restrict__ w1, const u16* __restrict__ w2, const u16* __restrict__ w3, const u16* __restrict__ w4,
             const u16* __restrict__ gw, u16* out, unsigned char* ws) {
    extern __shared__ __attribute__((aligned(16))) unsigned char smem[];
    const u32 tid = threadIdx.x, lane = tid & 63;
    const u32 w = __builtin_amdgcn_readfirstlane(tid >> 6);
    {
        const u32 rt0 = 8 * ((blockIdx.x & 7) >> 1) + (blockIdx.x >> 5);
        const rsrc_t rX = __builtin_amdgcn_make_buffer_rsrc((void*)x, 0, SEQ * DM * 2, 0x00020000);
        u32 l0 = lane; PIN(l0);
        const u32 xa = (rt0 * 64 + (l0 >> 3)) * 896 + (((l0 & 7) ^ (l0 >> 4)) * 16);
        const Pat pX = mkpat(xa, xa ^ 64, 8 * 896);
        dma_unit(rX, pX, (w >> 1) * 128 + (w & 1) * (32 * 896), w * 4096);
        if (w < 2) dma_unit(rX, pX, 6 * 128 + w * (32 * 896), (12 + w) * 4096);
    }
#define TOUCH(rs, off) asm volatile("s_mov_b32 m0, %0\n\ts_nop 0\n\tbuffer_load_dword %1, %2, 0 offen lds" ::"s"(MISC + 1536), "v"(off), "s"(rs) : "memory")
    if (w == 7) {
        u64 pc; int n0;
        asm volatile("s_getpc_b64 %0\n.Lfk_pc:\n\ts_mov_b32 %1, 0\n\ts_max_i32 %1, %1, .Lfk_e0-.Lfk_pc\n\ts_max_i32 %1, %1, .Lfk_e1-.Lfk_pc\n\ts_max_i32 %1, %1, .Lfk_e2-.Lfk_pc"
                     : "=s"(pc), "=s"(n0) :: "scc");
        const rsrc_t rC = __builtin_amdgcn_make_buffer_rsrc((void*)pc, 0, n0, 0x00020000);
        TOUCH(rC, lane < 12 ? ((blockIdx.x >> 3) + lane * 32) * 128 : 0x40000000u);
    }
    const u32 g = lane >> 4, c = lane & 15;
    const u32 b = blockIdx.x;
    const u32 rt = 8 * ((b & 7) >> 1) + (b >> 5), j = 4 * (b & 1) + ((b >> 3) & 3);
    const int row0 = rt * 64;
    PTA(0);
    const rsrc_t rWS = __builtin_amdgcn_make_buffer_rsrc((void*)ws, 0, WS_END, 0x00020000);
    const bool hasB = j < 7;
    const u32 psnS = hasB ? 0u : 0x40000000u;
    const u32 odd = j & 1;
#define DEF_TR() u32 lt_ = lane; PIN(lt_); const u32 q4 = (lt_ & 15) >> 2, pp = lt_ & 3; \
    const u32 trs = ((q4 >> 1) & 1) | (((lt_ >> 4) & 1) << 1), trb = (8 * (lt_ >> 4) + q4) * 128 + pp * 8
#define TRA(t) (trb + (((t) ^ trs) << 5))
    const u32x4 ONES = {0x3f803f80u, 0x3f803f80u, 0x3f803f80u, 0x3f803f80u};
    const u32x4 EMPTY = {0xffffffffu, 0xffffffffu, 0xffffffffu, 0xffffffffu};
#define LT(n) ((u32)(7 * ((n) + 1)))
    const u32 aC = L_PROG + (lane & 7) * 4;
    if (w == 10) sigv(L_PROG + (lane & 31) * 4, ((lane & 31) >= 16 && (lane & 31) < 24) ? 98 : ((lane & 31) >= 10 && (lane & 31) < 13 ? (lane & 31) + 4 : 0));
    const bool isLoader = w >= 8 && w != 10;
    if (!isLoader) { WAITV(0); BAR(); }

    auto finish_block = [&](u32 p) -> bool {
        const u32 t = p * 64 + lane;
        const u32 off = WS_RS + ((rt * 8 + j) * 8) * 7168 + t * 16;
        u32x4 q[8];
#pragma unroll
        for (int s = 0; s < 8; ++s) q[s] = ldg128(rWS, off + s * 7168);
        const u32 half = t & 1, r = (t >> 1) & 7, nn = t >> 4;
        const u32 row = 8 * j + r, kg = 2 * nn + half;
        const u32x4 x1res = ldg128(rWS, WS_X1 + rt * 57344 + (((row >> 4) * 14 + (kg >> 2)) * 64 + (kg & 3) * 16 + (row & 15)) * 16);
        const float og = 1.f - ldsf(L_GATE + row * 4);
        u32x4 o;
#pragma unroll
        for (int e = 0; e < 4; ++e) {
            f32x2 a = f32x2{blo(x1res[e]), bhi(x1res[e])} * og;
#pragma unroll
            for (int s = 0; s < 8; ++s) a += f32x2{blo(q[s][e]), bhi(q[s][e])};
            o[e] = pk2(a);
        }
        *(GAS u32x4*)((GAS unsigned char*)out + (size_t)(row0 + row) * 896 + nn * 32 + half * 16) = o;
        return true;
    };

    if (isLoader) {
        const rsrc_t rQKV = __builtin_amdgcn_make_buffer_rsrc((void*)wqkv, 0, DM * 3 * DM * 2, 0x00020000);
        const rsrc_t rWO = __builtin_amdgcn_make_buffer_rsrc((void*)wo, 0, DM * DM * 2, 0x00020000);
        const rsrc_t rW1 = __builtin_amdgcn_make_buffer_rsrc((void*)w1, 0, DM * DM * 2, 0x00020000);
        const rsrc_t rW2 = __builtin_amdgcn_make_buffer_rsrc((void*)w2, 0, DM * DM * 2, 0x00020000);
        const rsrc_t rW3 = __builtin_amdgcn_make_buffer_rsrc((void*)w3, 0, DM * FF * 2, 0x00020000);
        const rsrc_t rW4 = __builtin_amdgcn_make_buffer_rsrc((void*)w4, 0, DM * FF * 2, 0x00020000);
        const u32 l3 = w == 11 ? 2 : w - 8;
        u32 myL = L_LAND + l3 * 4, three = 3;
        PIN(myL); PIN(three);
        const u32 r8 = lane >> 3, sl = lane & 7;
        const u32 chA = sl ^ (((r8 >> 1) & 1) << 1), chB = chA ^ 4;
        const u32 jb = hasB ? j : 0;
        u32 rot = l3, pend = 0, seen = 0;
#define LANDED() asm volatile("s_mov_b64 exec, 1\n\tds_add_u32 %0, %1\n\ts_mov_b64 exec, -1" ::"v"(myL), "v"(three) : "memory")
#define POLL() do { u32 st_; asm volatile("s_getreg_b32 %0, hwreg(HW_REG_IB_STS)" : "=s"(st_)); \
            const u32 left_ = (((st_ & 15u) | ((st_ >> 18) & 48u)) + 3) >> 2; \
            _Pragma("unroll 1") while (pend > left_) { LANDED(); --pend; } } while (0)
#define ISSUED() do { ++pend; POLL(); } while (0)
#define FLUSH() do { _Pragma("unroll 1") while (pend > 0) { POLL(); SLEEP(1); } } while (0)
#define ROOM(need) do { if (__builtin_expect(!__all(seen >= (u32)(need)), 0)) { \
            _Pragma("unroll 1") for (u32 sp = 0; sp < (SPIN_CAP << 4); ++sp) { seen = peek(aC); if (__all(seen >= (u32)(need))) break; POLL(); SLEEP(1); } } seen = peek(aC); } while (0)
        u32 met = 0;
#define ROOMS(need) do { const u32 n_ = (need); if (n_ > met) { ROOM(n_ == 5 ? ((lane & 4) ? 0u : 6u) : n_); met = n_; } } while (0)
#define RUN7(k, nB, nA, rs, pat, so0, sstep, ent) do { ROOM((lane & 4) ? (nA) : (nB)); PTB(k); u32 u = rot; \
            _Pragma("unroll 1") for (; u < 7; u += 3) { dma_unit(rs, pat, (so0) + u * (sstep), (ent) + u * 4096); ISSUED(); } rot = u - 7; } while (0)
        const Pat pS = mkpat(r8 * 896 + chA * 16, r8 * 896 + chB * 16, 8 * 896);
        const Pat pQ = mkpat(r8 * 2688 + (chA >> 2) * 56 + (chA & 3) * 16, r8 * 2688 + (chB >> 2) * 56 + (chB & 3) * 16, 8 * 2688);
#define LD_STD(k, nB, nA, rs, half, ent) RUN7(k, nB, nA, rs, pS, (224 * (half)) * 896 + jb * 128, 32 * 896, ent)
#define LD_QKV(k, nB, nA, sec, half, ent) RUN7(k, nB, nA, rQKV, pQ, (224 * (half)) * 2688 + (sec) * 896 + 112 * j, 32 * 2688, ent)
        {
            u32 u = rot;
            dma_unit(rW1, pS, jb * 128 + u * (32 * 896), E2 + u * 4096); u += 3;
            dma_unit(rW1, pS, jb * 128 + u * (32 * 896), E2 + u * 4096); u += 3;
            WAITV(8);
            BAR();
            pend = 2;
#pragma unroll 1
            for (; u < 14; u += 3) { dma_unit(rW1, pS, jb * 128 + u * (32 * 896), E2 + u * 4096); ISSUED(); }
            rot = u - 14;
        }
        LD_QKV(3, 0, 0, 1, 0, E4);
        LD_QKV(4, 1, 1, 1, 1, E0);
        LD_QKV(5, 1, 1, 0, 0, E1);
        LD_QKV(6, 2, 2, 0, 1, E2);
        LD_STD(7, 3, 2, rW2, 0, E3);
        LD_STD(8, 0, 3, rW2, 1, E4);
        LD_QKV(9, 0, 4, 2, 0, E0);
        LD_QKV(10, 4, 0, 2, 1, E1);
        const Pat pT = mkpat(r8 * 3584 + chA * 16, r8 * 3584 + chB * 16, 8 * 3584);
        const Pat pU = mkpat((r8 + (chA >> 2) * 32) * 3584 + (chA & 3) * 16, (r8 + (chB >> 2) * 32) * 3584 + (chB & 3) * 16, 8 * 3584);
        const u32 s3f = 448 * j + odd * 64, s3h = 448 * j + (odd ? 0 : 384);
        const u32 pr = 16 * (r8 >> 2) + (r8 & 3);
        const Pat pP = mkpat(pr * 896 + chA * 16, pr * 896 + chB * 16, 4 * 896);
        const u32 s4f = (224 * j + odd * 32) * 896, s4l = (224 * j + (odd ? 0 : 192)) * 896, sWo = 56 * j * 896;
        PTB(11);
        {
            u64 tt = 0x1065a98765a987ull, tn = 0xc9555555110000ull, ts = 0x7060543201ull;
            u32 so = s3f;
#pragma unroll 1
            for (u32 ks = 0; ks < 14; ++ks) {
                ROOMS(5 + ((u32)tn & 15u));
                dma_unit(rW3, pT, so + rot * 128, ((u32)tt & 15u) * 12288 + rot * 4096); ++pend;
                if (ks % 3 == 2) POLL();
                const u32 si = (u32)ts & 15u;
                if (si) {
                    if (rot == 0) {
                        ROOMS(5 + ((0x5510000u >> (4 * (si - 1))) & 15u));
                        dma_unit(rW3, pU, s3h + (si - 1) * (64 * 3584), (u32)(0x22210e24232221ull >> (8 * (si - 1)) & 255u) * 4096); ++pend; rot = 2;
                    } else --rot;
                }
                tt >>= 4; tn >>= 4; ts >>= 4; so += 32 * 3584;
            }
        }
        PTB(12);
        {
            u64 tt = 0x1870365a2987ull, tn = 0xb99655432210ull;
            const u32 o0 = s4f + rot * (32 * 896), o2 = rot == 0 ? s4l : sWo + (rot - 1) * (28 * 896);
#pragma unroll 1
            for (u32 cb = 0; cb < 7; ++cb) {
                if (cb == 4) { tt = 0x365a29b4ull; tn = 0xedddcccbbull; }
#pragma unroll
                for (u32 t3 = 0; t3 < 3; ++t3) {
                    ROOMS(18 + ((u32)tn & 15u));
                    const u32 ld = ((u32)tt & 15u) * 12288 + rot * 4096;
                    if (t3 < 2) dma_unit(rW4, pP, o0 + t3 * (96 * 896) + cb * 128, ld);
                    else if (rot == 0) dma_unit(rW4, pP, o2 + cb * 128, ld);
                    else dma_unit(rWO, pP, o2 + cb * 128, ld);
                    ISSUED();
                    tt >>= 4; tn >>= 4;
                }
            }
        }
        FLUSH();
        wait_ge(L_HELP, 102);
        finish_block(l3);
        asm volatile(".Lfk_e0:");
    } else if (w == 10) {
        const rsrc_t rPHI = __builtin_amdgcn_make_buffer_rsrc((void*)wphi, 0, 16 * 28 * 28 * 2, 0x00020000);
        const rsrc_t rGW = __builtin_amdgcn_make_buffer_rsrc((void*)gw, 0, DM * 2, 0x00020000);
        const u32 head = 2 * j + (rt >> 4), chunk = rt & 15;
        {
            const u32 o_ = lane < 12 ? WS_MB1 + (lane >> 2) * 131072 + (rt * 8 + j) * 512 + (lane & 3) * 128 : 0x40000000u;
            asm volatile("s_mov_b32 m0, %0\n\ts_nop 0\n\tbuffer_load_dword %1, %2, 0 offen sc1 lds" ::"s"(MISC + 1536), "v"(o_), "s"(rWS) : "memory");
        }
        dma16(rGW, lane * 16, 0, GWL);
        dma_pair(rPHI, lane * 16, lane * 16, j * 3136, WPHI);
        dma_pair(rPHI, lane * 16, lane * 16, j * 3136 + 2048, WPHI + 2048);
        const u32 tag = __builtin_amdgcn_readfirstlane(__builtin_amdgcn_raw_buffer_load_b32(rWS, WS_EPOCH, 0, SC1)) + 1;
        const u32 taghi = tag * 0x9E3779B1u ^ 0x5bd1e995u;
        WAITV(0);
        sig(L_TAG, tag);
        sig(L_HELP, 1);
        const u32 me = rt * 8 + j;
#define PD0 28
#define PD1 8
#define POLL_ALL(off, first, gap) do { const u32 o_ = (off); SLEEP(first); _Pragma("unroll 1") for (u32 sp = 0; sp < SPIN_CAP; ++sp) { \
            u32x2 fl = ldg64(rWS, o_); if (__all(fl.x == tag && fl.y == taghi)) break; SLEEP(gap); } } while (0)
#define POLL_ALL2(off, first, gap) POLL_ALL(off, first, gap)
        {
            const int rw = (int)rt - 1 + (int)(lane >> 3);
            wait_ge(L_CNT, 4);
            POLL_ALL2(WS_MB1 + me * 512 + ((lane < 24 && (u32)rw < 32u) ? lane : 8 + (lane & 7)) * 8, PD0, PD1);
            sig(L_HELP, 100); PTA(3);
            wait_ge(L_CNT + 4, 4);
            POLL_ALL2(WS_MB1 + me * 512 + 256 + (lane & 7) * 8, PD0, PD1);
            sig(L_AUX, 100); PTA(4);
            POLL_ALL2(WS_MB3 + me * 512 + (lane & 31) * 8, 0, 12);
        }
        PTA(5);
        u32x4 pv[8];
        {
            const u32 poff = WS_KVP + (head * 32 + g) * 4096 + chunk * 256 + c * 16;
#pragma unroll
            for (int i = 0; i < 8; ++i) pv[i] = ldg128(rWS, poff + i * 16384);
            u32x4 o;
#pragma unroll
            for (int e = 0; e < 4; ++e) {
                f32x2 s = {0.f, 0.f};
#pragma unroll
                for (int i = 0; i < 8; ++i) s += f32x2{blo(pv[i][e]), bhi(pv[i][e])};
                o[e] = pk(xg_sum(s.x), xg_sum(s.y));
            }
            if (lane < 16) stg128(rWS, WS_KVF + head * 4096 + chunk * 256 + lane * 16, o);
            WAITV(0);
            if (lane < 32) stg64(rWS, WS_MB2 + (lane * 8 + j) * 512 + rt * 8, u32x2{tag, taghi});
        }
        PTA(6);
        POLL_ALL2(WS_MB2 + me * 512 + (lane & 31) * 8, PD0, 8);
#pragma unroll
        for (int i = 0; i < 8; ++i) pv[i] = ldg128(rWS, WS_KVF + 2 * j * 4096 + lane * 16 + i * 1024);
        wait_ge(aC, (lane & 4) ? 5 : 7);
#pragma unroll
        for (int i = 0; i < 8; ++i) sts128(MR + i * 1024 + lane * 16, pv[i]);
        sig(L_HELP, 101);
        PTA(7);
        wait_ge(L_CNT + 16, 8);
        POLL_ALL2(WS_MB2 + me * 512 + 384 + (lane & 7) * 8, 12, 2);
        sig(L_HELP, 102);
        PTA(8);
        wait_ge(L_CNT + 12, 8);
        POLL_ALL2(WS_MB2 + me * 512 + 256 + (lane & 7) * 8, 8, 1);
        sig(L_HELP, 103);
        PTA(9);
        asm volatile(".Lfk_e1:");
        if (b == 0 && lane == 0) __builtin_amdgcn_raw_buffer_store_b32(tag, rWS, WS_EPOCH, 0, SC1);
    } else {
        const u32 mt = w & 3, hf = w >> 2;
        const u32 myrow = 16 * mt + c;
        DEF_TR();
        const u32 myP = L_PROG + w * 4;
#define SIGP(n) sig(myP, (n))
        const u32 aS = L_PROG + (c < 13 ? c : 0) * 4;
        u32 sv = 0;
#define NEEDL(n, id) do { if (__builtin_expect(!__all(c < 10 || c > 12 || sv >= LT(n)), 0)) { PTA(id); snap_wait(aS, (c >= 10 && c <= 12) ? LT(n) : 0u, sv); PTB(id); } } while (0)
#define NEEDW(wd, n, id) do { if (__builtin_expect(rl(sv, wd) < (u32)(n), 0)) { PTA(id); snap_wait(aS, c == (wd) ? (u32)(n) : 0u, sv); PTB(id); } } while (0)
#define NEEDP(n, id) do { if (__builtin_expect(!__all(c >= 8 || sv >= (u32)(n)), 0)) { PTA(id); snap_wait(aS, c < 8 ? (u32)(n) : 0u, sv); PTB(id); } } while (0)
#define PUBLISH(word, n, off) do { WAITV(0); PUBLISH_(word, n, off); } while (0)
#define PUBLISH_(word, n, off) do { u32 o_; \
            asm volatile("s_mov_b64 exec, 1\n\tds_add_rtn_u32 %0, %1, %2\n\ts_mov_b64 exec, -1\n\ts_waitcnt lgkmcnt(0)" : "=&v"(o_) : "v"(L_CNT + (word) * 4), "v"(1u) : "memory"); \
            if (__builtin_amdgcn_readfirstlane(o_) == (n) - 1) { const u32 tg_ = peek(L_TAG); stg64(rWS, (off), u32x2{tg_, tg_ * 0x9E3779B1u ^ 0x5bd1e995u}); } } while (0)
#define NEEDT(nB, nA, id) do { const u32 t_ = c < 4 ? (u32)(nB) : (c < 8 ? (u32)(nA) : 0u); if (__builtin_expect(!__all(sv >= t_), 0)) { PTA(id); snap_wait(aS, t_, sv); PTB(id); } } while (0)
        const u32 tr[4] = {TRA(0), TRA(1), TRA(2), TRA(3)};
        u32 hoff[2], x1o[2], zoff[2];
#pragma unroll
        for (int p2 = 0; p2 < 2; ++p2) {
            const u32 nt = 2 * p2 + (g & 1);
            const u32 grp = (4 * j + nt) / 7;
            const int dlt = grp == 0 ? -1 : (grp == 1 ? 1 : (grp == 2 ? -2 : 2));
            const u32 uu = 2 * nt + (g >> 1);
            const u32 cpart = (((2 * j + (uu >> 2)) * 64 + (uu & 3) * 16) * 16) | psnS;
            const int R = row0 + myrow, Rd = R - dlt;
            hoff[p2] = (u32)Rd < (u32)SEQ ? WS_H + cpart + (Rd >> 4) * (14 * 1024) + (Rd & 15) * 16 : 0xfffffff0u;
            const u32 own = cpart + (R >> 4) * (14 * 1024) + (R & 15) * 16;
            x1o[p2] = WS_X1 + own;
            zoff[p2] = (u32)(R + dlt) < (u32)SEQ ? 0xfffffff0u : WS_H + own;
        }
        if (w == 6) {
        {
            const rsrc_t rQKV = __builtin_amdgcn_make_buffer_rsrc((void*)wqkv, 0, DM * 3 * DM * 2, 0x00020000);
            const rsrc_t rWO = __builtin_amdgcn_make_buffer_rsrc((void*)wo, 0, DM * DM * 2, 0x00020000);
            const rsrc_t rW2 = __builtin_amdgcn_make_buffer_rsrc((void*)w2, 0, DM * DM * 2, 0x00020000);
            const rsrc_t rW3 = __builtin_amdgcn_make_buffer_rsrc((void*)w3, 0, DM * FF * 2, 0x00020000);
            const rsrc_t rW4 = __builtin_amdgcn_make_buffer_rsrc((void*)w4, 0, DM * FF * 2, 0x00020000);
            TOUCH(rQKV, lane < 37 ? (b * 37 + lane) * 128 : 0x40000000u);
            TOUCH(rW2, lane < 13 ? (b * 13 + lane) * 128 : 0x40000000u);
            TOUCH(rW3, lane < 49 ? (b * 49 + lane) * 128 : 0x40000000u);
            TOUCH(rW4, lane < 49 ? (b * 49 + lane) * 128 : 0x40000000u);
            TOUCH(rWO, lane < 13 ? (b * 13 + lane) * 128 : 0x40000000u);
        }
        }
#define LOAD_XF() u32x4 xf[14]; { const u32 sx = (myrow >> 1) & 7, a0 = myrow * 128 + ((g ^ sx) * 16), a1 = a0 ^ 64; \
            _Pragma("unroll") for (int ks = 0; ks < 14; ++ks) xf[ks] = lds128((ks >> 1) * XT_CB + ((ks & 1) ? a1 : a0)); }
#define LOAD_WPF() u32x4 wpf[4]; NEEDW(8, 1, 30); _Pragma("unroll") for (int q = 0; q < 4; ++q) { \
            const u32 a = WPHI + (q >> 1) * 1568 + (4 * g + q4) * 56 + (q & 1) * 32 + pp * 8; u32x4 t = ldw2(a, a + 16 * 56); if (g == 3) { t.z = 0; t.w = 0; } wpf[q] = t; }
        constexpr u32 G_OFF[14] = {86016, 98304, 110592, 122880, 61440, 73728, 86016, 98304, 110592, 122880, 61440, 73728, 0, 12288};
        constexpr u32 S_OFF[7] = {135168, 139264, 143360, 147456, 57344, 135168, 139264};
        constexpr u32 G_LAND[14] = {87, 91, 94, 98, 102, 106, 110, 113, 117, 120, 124, 127, 130, 133};
        constexpr u32 S_LAND[7] = {88, 95, 99, 103, 107, 114, 121};
        constexpr u32 H_OFF[21] = {86016, 98304, 110592, 24576, 122880, 61440, 73728, 36864, 0, 86016, 98304, 12288, 49152, 135168, 110592, 24576, 122880, 61440, 73728, 36864, 0};
        u32x4 phq[2][2];
        if (hf == 0) {
            if (rt == 0 || rt == 31) { stg128(rWS, zoff[0], u32x4{0, 0, 0, 0}); stg128(rWS, zoff[1], u32x4{0, 0, 0, 0}); }
            LOAD_XF();
            u32x2 xres[4];
            {
                const u32 sx = (myrow >> 1) & 7;
#pragma unroll
                for (int t = 0; t < 4; ++t) xres[t] = lds64((hasB ? j : 0) * XT_CB + myrow * 128 + (((2 * t + (g >> 1)) ^ sx) * 16) + (g & 1) * 8);
            }
            SIGP(1);
            {
                u32x4 on1 = ONES; PIN(on1);
                f32x4 s0, s1;
#pragma unroll
                for (int ks = 0; ks < 14; ++ks) { MFMA_K(ks == 0, s0, on1, xf[ks]); MFMA_K(ks == 0, s1, xf[ks], xf[ks]); }
                MFMA_DONE2(s0, s1);
                const float mean = s0[0] * (1.f / DM);
                const float var = fmaxf(xg_sum(g == (c >> 2) ? diag4(s1, c) : 0.f) * (1.f / DM) - mean * mean, 0.f);
                const float rstd = __builtin_amdgcn_rsqf(var + 1e-5f);
                const float k1 = -GC1 * rstd, k2 = -GC2 * rstd * rstd * rstd;
                f32x4 a1c[4];
                NEEDL(2, 2);
                proj7<0>(E2, tr, xf, a1c, aS, sv);
                SIGP(2);
                NEEDL(3, 3);
                proj7<7>(E3, tr, xf, a1c, aS, sv);
                SIGP(3);
                NEEDT(0, 2, 4);
                u32 pkd[4][2];
#pragma unroll
                for (int t = 0; t < 4; ++t) {
                    const f32x4 cs = __builtin_bit_cast(f32x4, lds128(L_CS1 + (16 * t + 4 * g) * 4));
                    f32x2 y0 = f32x2{a1c[t][0], a1c[t][1]} - mean * f32x2{cs[0], cs[1]}, y1 = f32x2{a1c[t][2], a1c[t][3]} - mean * f32x2{cs[2], cs[3]};
                    pkd[t][0] = (pk2(gelu2(y0, k1, k2, rstd))); pkd[t][1] = (pk2(gelu2(y1, k1, k2, rstd)));
                    if (t & 1) stg128(rWS, hoff[t >> 1], swap_units(pkd[t - 1][0], pkd[t - 1][1], pkd[t][0], pkd[t][1]));
                }
            }
            PTA(34);
            {
                NEEDW(8, 1, 30);
                const int rc = (int)rt - 1 + (int)(lane >> 3);
                PUBLISH(0, 4, (lane < 24 && (u32)rc < 32u) ? WS_MB1 + (rc * 8 + (lane & 7)) * 512 + ((2 - (lane >> 3)) * 8 + j) * 8 : 0xfffffff0u);
            }
            f32x4 aq[4];
            NEEDL(6, 5);
            proj7<0>(E1, tr, xf, aq, aS, sv);
            SIGP(4);
            NEEDL(7, 6);
            proj7<7>(E2, tr, xf, aq, aS, sv);
            SIGP(5);
            u32x4 hfr[14];
            const u32 goff = (WS_H + rt * 57344 + (mt * 14 * 64 + lane) * 16) | psnS;
            NEEDW(8, 100, 7);
#pragma unroll
            for (int i = 0; i < 14; ++i) hfr[i] = ldg128(rWS, goff + i * 1024);
            PTA(35);
            f32x4 a2[4];
            NEEDL(9, 8);
            proj7<0>(E3, tr, hfr, a2, aS, sv);
            proj7<7>(E4, tr, hfr, a2, aS, sv);
            u32 pkd[4][2];
#pragma unroll
            for (int t = 0; t < 4; ++t) {
                pkd[t][0] = (pk(a2[t][0] + blo(xres[t].x), a2[t][1] + bhi(xres[t].x)));
                pkd[t][1] = (pk(a2[t][2] + blo(xres[t].y), a2[t][3] + bhi(xres[t].y)));
                if (t & 1) stg128(rWS, x1o[0] + (t >> 1) * 1024, swap_units(pkd[t - 1][0], pkd[t - 1][1], pkd[t][0], pkd[t][1]));
            }
            PTA(36);
            SIGP(6);
            PUBLISH(1, 4, lane < 8 ? WS_MB1 + (rt * 8 + lane) * 512 + 256 + j * 8 : 0xfffffff0u);
            {
                LOAD_WPF();
                SIGP(7);
                features(aq[0], aq[1], wpf, g, phq[0][0], phq[0][1]);
                features(aq[2], aq[3], wpf + 2, g, phq[1][0], phq[1][1]);
                PIN(phq[0][0]); PIN(phq[0][1]); PIN(phq[1][0]); PIN(phq[1][1]);
            }
        } else {
            LOAD_XF();
            SIGP(1);
            {
                u32 ca = E2 + TRA(mt); PIN(ca);
                f32x4 cs = {};
                NEEDL(3, 2);
#pragma unroll
                for (int k = 0; k < 14; ++k) cs = mfma(ldw(ca + k * 4096), ONES, cs);
                if (c == 0) sts128(L_CS1 + (16 * mt + 4 * g) * 4, __builtin_bit_cast(u32x4, cs));
                SIGP(2);
            }
            {
                NEEDW(8, 1, 30);
                f32x4 s0 = {};
#pragma unroll
                for (int ks = 0; ks < 14; ++ks) s0 = mfma(lds128(GWL + ks * 64 + g * 16), xf[ks], s0);
                if (g == 0) stsf(L_GATE + myrow * 4, __builtin_amdgcn_rcpf(1.f + __builtin_amdgcn_exp2f(-LOG2E * s0[0])));
            }
            const u32 wsw = ((c >> 1) & 1) | (((c >> 3) & 1) << 1);
            const u32 wrb = E2 + myrow * 128 + g * 8;
            {
                f32x4 ak[4];
                NEEDL(4, 5);
                proj7<0>(E4, tr, xf, ak, aS, sv);
                SIGP(3);
                NEEDL(5, 6);
                proj7<7>(E0, tr, xf, ak, aS, sv);
                SIGP(4);
                LOAD_WPF();
                u32x4 pos[2], neg[2];
                features(ak[0], ak[1], wpf, g, pos[0], neg[0]);
                features(ak[2], ak[3], wpf + 2, g, pos[1], neg[1]);
                NEEDT(5, 0, 32);
#pragma unroll
                for (int hd = 0; hd < 2; ++hd) {
                    *(LAS u32x2*)(wrb + hd * 8192 + ((0 ^ wsw) << 5)) = u32x2{pos[hd].x, pos[hd].y};
                    *(LAS u32x2*)(wrb + hd * 8192 + ((1 ^ wsw) << 5)) = u32x2{pos[hd].z, pos[hd].w};
                    *(LAS u32x2*)(wrb + hd * 8192 + ((2 ^ wsw) << 5)) = u32x2{neg[hd].x, neg[hd].y};
                    *(LAS u32x2*)(wrb + hd * 8192 + ((3 ^ wsw) << 5)) = u32x2{neg[hd].z, neg[hd].w};
                }
            }
            PTA(34);
            {
                f32x4 av[4];
                NEEDL(10, 10);
                proj7<0>(E0, tr, xf, av, aS, sv);
                NEEDL(11, 11);
                proj7<7>(E1, tr, xf, av, aS, sv);
#pragma unroll
                for (int t = 0; t < 4; ++t) {
                    u32x2 vv = {pk(av[t][0], av[t][1]), pk(av[t][2], av[t][3])};
                    if ((t & 1) && g == 3) vv = u32x2{0x00003f80u, 0u};
                    *(LAS u32x2*)(wrb + 16384 + ((t ^ wsw) << 5)) = vv;
                }
            }
            SIGP(5);
            PTA(35);
            NEEDT(0, 5, 13);
            {
                const u32 pa = E2 + TRA(2 * (mt >> 1)), pb = E2 + TRA(2 * (mt >> 1) + 1);
                u32x4 kp[2];
#pragma unroll
                for (int hd = 0; hd < 2; ++hd) {
                    f32x4 kv[2] = {};
                    const u32 va = E2 + 16384 + TRA(2 * hd + (mt & 1));
#pragma unroll
                    for (int ks = 0; ks < 2; ++ks) {
                        const u32x4 vf = ldw(va + ks * 4096);
                        kv[0] = mfma(ldw(pa + hd * 8192 + ks * 4096), vf, kv[0]);
                        kv[1] = mfma(ldw(pb + hd * 8192 + ks * 4096), vf, kv[1]);
                    }
                    const u32x4 t = pk8(kv[0], kv[1]);
                    kp[hd] = t;
                }
                SIGP(6);
#pragma unroll
                for (int hd = 0; hd < 2; ++hd) stg128(rWS, WS_KVP + ((2 * j + hd) * 32 + rt) * 4096 + mt * 1024 + lane * 16, kp[hd]);
                PUBLISH(2, 4, lane < 32 ? WS_MB3 + (lane * 8 + j) * 512 + rt * 8 : 0xfffffff0u);
            }
            PTA(36);
        }
        u32x4 pre[6][2];
        {
            u32x4 xv[7];
            const u32 x1off = WS_X1 + rt * 57344 + ((mt * 14 + 7 * hf) * 64 + lane) * 16;
            NEEDW(9, 100, 12);
#pragma unroll
            for (int i = 0; i < 7; ++i) xv[i] = ldg128(rWS, x1off + i * 1024);
            NEEDT(0, 5, 15);
#pragma unroll
            for (int i = 0; i < 7; ++i) sts128(((7 * hf * 4 + mt) * 64 + lane) * 16 + i * 4096, xv[i]);
            PTA(37);
        }
        {
            { u32 l_ = lane; PIN(l_); PTA(14); wait_ge(L_LAND + ((l_ & 3) == 3 ? 0 : (l_ & 3)) * 4, G_LAND[5]); PTB(14); }
            const bool w6 = w == 6;
            const u32 ub = w6 ? 0 : ((w >> 1) % 3) * 4096;
            const u32 e0 = ub + TRA(2 * (w & 1)), e1 = ub + TRA(2 * (w & 1) + 1), o0 = w6 ? TRA(2) : e0, o1 = w6 ? TRA(3) : e1;
#pragma unroll
            for (int ks = 0; ks < 6; ++ks) {
                const u32 off = w6 ? S_OFF[ks >> 1] : G_OFF[ks];
                pre[ks][0] = ldw(((ks & 1) ? o0 : e0) + off); pre[ks][1] = ldw(((ks & 1) ? o1 : e1) + off);
            }
        }
        SIGP(11);
        u32 ln = lane; PIN(ln);
        const u32 g2 = ln >> 4, c2 = ln & 15;
        const u32 trs2 = ((ln >> 3) & 1) | ((g2 & 1) << 1);
        const u32 trb2 = (8 * g2 + (c2 >> 2)) * 128 + (ln & 3) * 8;
#define TRB(t) (trb2 + (((t) ^ trs2) << 5))
        const u32 aC2 = L_PROG + (ln & 7) * 4, aL2 = L_LAND + (c2 < 3 ? c2 : 0) * 4;
        const u32 mh4 = w & 1, nt4 = w >> 1;
        u32 rsoff;
        {
            const u32 row = 16 * (2 * mh4 + (g2 & 1)) + c2;
            rsoff = WS_RS + ((rt * 8 + (row >> 3)) * 8 + j) * 7168 + (nt4 * 8 + (row & 7)) * 32 + (g2 >> 1) * 16;
        }
        constexpr u32 B_LAND[7] = {142, 151, 160, 169, 178, 187, 196};
        constexpr u32 AF_OFF[3] = {12288, 49152, 135168};

        const u32 xb = ln * 16;
        u32 wb4[3];
        wb4[0] = TRB(nt4); wb4[1] = wb4[0] + 61440; wb4[2] = wb4[0] + 122880;
        #define W4_OFF(cb, k) (H_OFF[3 * (cb) + (k) / 3] + ((k) % 3) * 4096)
#define W4_RD(cb, k) ldw(wb4[W4_OFF(cb, k) / 61440] + W4_OFF(cb, k) % 61440)
        u32x4 f4[2][7];
#define S4_PRE() do { wait_ge(aL2, B_LAND[0]); _Pragma("unroll") for (int k = 0; k < 7; ++k) f4[0][k] = W4_RD(0, k); SIGP(27); } while (0)
#define SKEW 2
        if (w < 7) {
            f32x4 a3[4][2], a3s[2];
            u32x4 f[2][2], xr[2][4], on3 = ONES;
            u32 pa = ln == 0 ? myP : (ln == 1 ? myP + 64 : MISC + 1536 + ln * 4), two = ln == 1 ? 8 : 2;
            const u32 aQ = L_PROG + (c2 < 7 ? 16 + c2 : (c2 < 10 ? c2 + 3 : 10)) * 4;
            PIN(on3); PIN(pa); PIN(two);
#define S3_F(ks, t) ((ks) < 6 ? pre[(ks) < 6 ? (ks) : 0][t] : f[(ks) & 1][t])
#define S3_RX(ks, m) xr[(ks) & 1][m] = lds128(xb + ((ks) * 4 + (m)) * 1024)
#define S3_LOOP(RW, LAND) do { \
            PTA(16); wait_ge(aC2, 11); PTB(16); \
            u32 lv = 0; \
            S3_RX(0, 0); S3_RX(0, 1); S3_RX(0, 2); S3_RX(0, 3); \
            SB(); \
            _Pragma("unroll") for (int ks = 0; ks < 14; ++ks) { \
                const int kn = ks < 13 ? ks + 1 : 13; \
                if ((ks & 1) && ks < 13) { if (__builtin_expect(!__all(lv >= (u32)(98 + 4 * (ks - 1))), 0)) { PTA(39); wait_ge(aQ, 98 + 4 * (ks - 1)); PTB(39); } } \
                MFMA_K(ks == 0, a3s[0], S3_F(ks, 0), on3); \
                if (ks < 13 && kn >= 6) RW(kn, 0); \
                SB(); \
                MFMA_K(ks == 0, a3s[1], S3_F(ks, 1), on3); \
                if (ks < 13 && kn >= 6) RW(kn, 1); \
                SB(); \
                _Pragma("unroll") for (int m = 0; m < 4; ++m) { \
                    MFMA_K(ks == 0, a3[m][0], S3_F(ks, 0), xr[ks & 1][m]); \
                    if (ks < 13) S3_RX(kn, m); \
                    SB(); \
                    MFMA_K(ks == 0, a3[m][1], S3_F(ks, 1), xr[ks & 1][m]); \
                    if (m == 1 && !(ks & 1)) lv = peek(aQ); \
                    if (m == 3 && !(ks & 1)) asm volatile("ds_add_u32 %0, %1" ::"v"(pa), "v"(two) : "memory"); \
                    SB(); \
                } \
            } } while (0)
            {
                const bool w6 = w == 6;
                u32 wa[2][2];
                wa[0][0] = (w6 ? 0 : (w >> 1) * 4096) + TRB(2 * (w & 1)); wa[0][1] = (w6 ? 0 : (w >> 1) * 4096) + TRB(2 * (w & 1) + 1);
                wa[1][0] = w6 ? TRB(2) : wa[0][0]; wa[1][1] = w6 ? TRB(3) : wa[0][1];
                PIN(wa[0][0]); PIN(wa[0][1]); PIN(wa[1][0]); PIN(wa[1][1]);
#define S3_RWG(ks, t) do { u32 a_ = wa[(ks) & 1][t] + (w6 ? S_OFF[(ks) >> 1] : G_OFF[ks]); PIN(a_); f[(ks) & 1][t] = ldw(a_); } while (0)
                S3_LOOP(S3_RWG, 0);
            }
            asm volatile("s_nop 7" ::: "memory");
            SIGP(26);
            PIN(wb4[0]); PIN(wb4[1]); PIN(wb4[2]);
            S4_PRE();
            PTA(17); wait_ge(aC2, 26); PTB(17);
            float mean[4], k1[4], k2[4], so[4];
#pragma unroll
            for (int m = 0; m < 4; ++m) {
                const u32 row = 16 * m + c2;
                mean[m] = ldsf(L_SUM1 + row * 4) * (1.f / DM);
                const float var = fmaxf(ldsf(L_SQ1 + row * 4) * (1.f / DM) - mean[m] * mean[m], 0.f);
                const float rstd = __builtin_amdgcn_rsqf(var + 1e-5f);
                k1[m] = -GC1 * rstd; k2[m] = -GC2 * rstd * rstd * rstd; so[m] = rstd * (1.f - ldsf(L_GATE + row * 4));
            }
            const u32 hdst = (w < 3 ? AF_OFF[0] : (w < 6 ? AF_OFF[1] : AF_OFF[2])) + (w % 3) * 4096 + ln * 16;
#pragma unroll
            for (int m = 0; m < 4; ++m) {
                u32x4 o;
#pragma unroll
                for (int t = 0; t < 2; ++t) {
                    f32x2 y0 = f32x2{a3[m][t][0], a3[m][t][1]} - mean[m] * f32x2{a3s[t][0], a3s[t][1]};
                    f32x2 y1 = f32x2{a3[m][t][2], a3[m][t][3]} - mean[m] * f32x2{a3s[t][2], a3s[t][3]};
                    o[2 * t] = pk2(gelu2(y0, k1[m], k2[m], so[m])); o[2 * t + 1] = pk2(gelu2(y1, k1[m], k2[m], so[m]));
                }
                sts128(hdst + m * 1024, o);
            }
        } else {
            f32x4 s1[4], s2[4];
            u32x4 v[2][4], on7 = ONES;
            PIN(on7);
            wait_ge(aC2, 11);
#pragma unroll
            for (int q = 0; q < 4; ++q) v[0][q] = lds128(xb + q * 1024);
            SB();
#pragma unroll
            for (int ks = 0; ks < 14; ++ks) {
#pragma unroll
                for (int q = 0; q < 4; ++q) {
                    MFMA_K(ks == 0, s1[q], on7, v[ks & 1][q]);
                    if (ks < 13) v[(ks + 1) & 1][q] = lds128(xb + ((ks + 1) * 4 + q) * 1024);
                    SB();
                    MFMA_K(ks == 0, s2[q], v[ks & 1][q], v[ks & 1][q]);
                }
                if (!(ks & 1)) SIGP(13 + ks);
            }
            MFMA_DONE2(s1[3], s2[3]);
            if (g2 == (c2 >> 2)) {
#pragma unroll
                for (int m = 0; m < 4; ++m) { stsf(L_SUM1 + (16 * m + c2) * 4, s1[m][0]); stsf(L_SQ1 + (16 * m + c2) * 4, diag4(s2[m], c2)); }
            }
            SIGP(26);
            PIN(wb4[0]); PIN(wb4[1]); PIN(wb4[2]);
            S4_PRE();
            wait_ge(aC2, 26);
        }
        u32x4 af[2][9];
        if (hf == 0) {
            PTA(18); wait_ge(L_HELP, 101); PTB(18);
            const float gt = ldsf(L_GATE + (16 * mt + c2) * 4);
#pragma unroll
            for (int hd = 0; hd < 2; ++hd) {
                u32x4 kf[4];
#pragma unroll
                for (int i = 0; i < 4; ++i) kf[i] = lds128(MR + (hd * 4 + i) * 1024 + ln * 16);
                f32x4 o0 = mfma(kf[0], phq[hd][0], f32x4{}); o0 = mfma(kf[2], phq[hd][1], o0);
                f32x4 o1 = mfma(kf[1], phq[hd][0], f32x4{}); o1 = mfma(kf[3], phq[hd][1], o1);
                const float den = xg_sum(g2 == 3 ? o1[0] : 0.f) + 1e-6f;
                const float sc = gt * __builtin_amdgcn_rcpf(den);
                u32x4 f = pk8(o0 * sc, o1 * sc);
                if (g2 == 3) { f.z = 0; f.w = 0; }
                sts128(AF_OFF[2] + (1 + hd) * 4096 + mt * 1024 + ln * 16, f);
            }
        }
        SIGP(28);
        {
            PTA(19); wait_ge(aC2, 28); PTB(19);
            {
                u32 ab = (2 * mh4) * 1024 + ln * 16, ab2 = ab + AF_OFF[2];
                PIN(ab); PIN(ab2);
#pragma unroll
                for (int k = 0; k < 9; ++k)
#pragma unroll
                    for (int m = 0; m < 2; ++m) af[m][k] = lds128((k < 6 ? ab + AF_OFF[k / 3] : ab2) + (k % 3) * 4096 + m * 1024);
            }
            SIGP(29);
            u32 lv = peek(aL2);
#pragma unroll
            for (int cb = 1; cb < 8; ++cb) {
                const int p = cb - 1;
                if (cb < 7) { if (__builtin_expect(!__all(lv >= B_LAND[cb < 7 ? cb : 0]), 0)) { PTA(20 + cb); wait_ge(aL2, B_LAND[cb < 7 ? cb : 0]); PTB(20 + cb); } }
                const u32x4 wo0 = W4_RD(p, 7), wo1 = W4_RD(p, 8);
                f32x4 a4[2];
#pragma unroll
                for (int k = 0; k < 7; ++k) {
                    if (k == 0) asm volatile("v_mfma_f32_16x16x32_bf16 %0, %1, %2, 0" : "=&v"(a4[0]) : "v"(f4[p & 1][0]), "v"(af[0][0]));
                    else MFMA_ACC(a4[0], f4[p & 1][k], af[0][k]);
                    if (cb < 7) f4[cb & 1][k] = W4_RD(cb < 7 ? cb : 0, k);
                    SB();
                    if (k == 0) asm volatile("v_mfma_f32_16x16x32_bf16 %0, %1, %2, 0" : "=&v"(a4[1]) : "v"(f4[p & 1][0]), "v"(af[1][0]));
                    else MFMA_ACC(a4[1], f4[p & 1][k], af[1][k]);
                    if (k == 3) lv = peek(aL2);
                    if (cb <= 3 && k == 5) SIGP(29 + cb);
                    SB();
                }
                MFMA_ACC(a4[0], wo0, af[0][7]); MFMA_ACC(a4[1], wo0, af[1][7]);
                MFMA_ACC(a4[0], wo1, af[0][8]); MFMA_ACC(a4[1], wo1, af[1][8]);
                asm volatile("s_nop 7" : "+v"(a4[0]), "+v"(a4[1]));
                u32x4 u = swap_units(pk(a4[0][0], a4[0][1]), pk(a4[0][2], a4[0][3]), pk(a4[1][0], a4[1][1]), pk(a4[1][2], a4[1][3]));
                stg128(rWS, rsoff + p * 1024, u);
                if (cb == 5) { WAITV(1); PUBLISH_(4, 8, ln < 8 ? WS_MB2 + (rt * 8 + ln) * 512 + 384 + j * 8 : 0xfffffff0u); }
                SB();
            }
        }
        PTA(27);
        PUBLISH(3, 8, ln < 8 ? WS_MB2 + (rt * 8 + ln) * 512 + 256 + j * 8 : 0xfffffff0u);
        PTA(28);
        wait_ge(L_HELP, w == 7 ? 102 : 103);
        PTA(29);
        asm volatile(".Lfk_e2:");
        if (w >= 4) finish_block(w == 7 ? 3 : w);
    }
}
}

extern "C" void kernel_launch(void* const* d_in, const int* in_sizes, int n_in, void* d_out, int out_size, void* d_ws, size_t ws_size,
                              hipStream_t stream) {
    (void)hipFuncSetAttribute((const void*)fused_kernel, hipFuncAttributeMaxDynamicSharedMemorySize, LDS_BYTES);
    fused_kernel<<<256, NWAVE * 64, LDS_BYTES, stream>>>((const u16*)d_in[0], (const u16*)d_in[1], (const u16*)d_in[2], (const u16*)d_in[3],
                                                         (const u16*)d_in[5], (const u16*)d_in[6], (const u16*)d_in[7], (const u16*)d_in[8],
                                                         (const u16*)d_in[9], (u16*)d_out, (unsigned char*)d_ws);
}
```

```cpp
#include <hip/hip_runtime.h>
#include <hip/hip_bf16.h>

namespace {
typedef unsigned short u16;
typedef unsigned u32;
typedef unsigned long long u64;
typedef short bf16x8 __attribute__((ext_vector_type(8)));
typedef short s16x4 __attribute__((ext_vector_type(4)));
typedef float f32x4 __attribute__((ext_vector_type(4)));
typedef float f32x2 __attribute__((ext_vector_type(2)));
typedef __bf16 bf16x2 __attribute__((ext_vector_type(2)));
typedef u32 u32x4 __attribute__((ext_vector_type(4)));
typedef u32 u32x2 __attribute__((ext_vector_type(2)));
#define LAS __attribute__((address_space(3)))
#define GAS __attribute__((address_space(1)))
#define DI __device__ __forceinline__
typedef __amdgpu_buffer_rsrc_t rsrc_t;

constexpr int SEQ = 2048, DM = 448, FF = 1792;

constexpr u32 E0 = 0, E1 = 28672, E2 = 57344, E3 = 86016, E4 = 114688, XX = 143360, MR = 151552, MISC = 159744;
constexpr u32 LDS_BYTES = 163840;
constexpr u32 L_SUM = MISC, L_SQ = MISC + 256, L_GATE = MISC + 512, L_SUM1 = MISC + 768, L_SQ1 = MISC + 1024, L_CS1 = MISC + 3072, GWL = MISC + 2048;
constexpr u32 WPHI = MR;
constexpr u32 XT_CB = 8192;

constexpr u32 WS_EPOCH = 0;
constexpr u32 WS_H = 65536;
constexpr u32 WS_X1 = WS_H + 32 * 57344;
constexpr u32 WS_KVP = WS_X1 + 32 * 57344;
constexpr u32 WS_KVF = WS_KVP + 16 * 32 * 8192;
constexpr u32 WS_RS = WS_KVF + 16 * 4096;
constexpr u32 WS_MB1 = WS_RS + 32 * 64 * 7168;
constexpr u32 WS_MB2 = WS_MB1 + 131072;
constexpr u32 WS_MB3 = WS_MB2 + 131072;
constexpr u32 WS_END = WS_MB3 + 131072;
constexpr u32 SPIN_CAP = 1u << 16;
constexpr int SC1 = 16;

DI f32x4 mfma(u32x4 a, u32x4 b, f32x4 c) {
    return __builtin_amdgcn_mfma_f32_16x16x32_bf16(__builtin_bit_cast(bf16x8, a), __builtin_bit_cast(bf16x8, b), c, 0, 0, 0);
}
DI u32 pk(float a, float b) { f32x2 f = {a, b}; return __builtin_bit_cast(u32, __builtin_convertvector(f, bf16x2)); }
DI u32 pk2(f32x2 f) { return __builtin_bit_cast(u32, __builtin_convertvector(f, bf16x2)); }
DI u32x4 pk8(f32x4 a, f32x4 b) { return u32x4{pk(a[0], a[1]), pk(a[2], a[3]), pk(b[0], b[1]), pk(b[2], b[3])}; }
DI float blo(u32 v) { return __builtin_bit_cast(float, v << 16); }
DI float bhi(u32 v) { return __builtin_bit_cast(float, v & 0xffff0000u); }
DI u32x4 lds128(u32 a) { return *(LAS const u32x4*)a; }
DI u32x2 lds64(u32 a) { return *(LAS const u32x2*)a; }
DI float ldsf(u32 a) { return *(LAS const float*)a; }
DI void sts128(u32 a, u32x4 v) { *(LAS u32x4*)a = v; }
DI void stsf(u32 a, float v) { *(LAS float*)a = v; }
DI u32x4 ldw2(u32 a0, u32 a1) {
    s16x4 lo = __builtin_amdgcn_ds_read_tr16_b64_v4i16((LAS s16x4*)a0);
    s16x4 hi = __builtin_amdgcn_ds_read_tr16_b64_v4i16((LAS s16x4*)a1);
    u32x2 l = __builtin_bit_cast(u32x2, lo), h = __builtin_bit_cast(u32x2, hi);
    return u32x4{l.x, l.y, h.x, h.y};
}
DI u32x4 ldw(u32 a) { return ldw2(a, a + 512); }
DI void dma16(rsrc_t rs, u32 voff, u32 soff, u32 lds) {
    asm volatile("s_mov_b32 m0, %0\n\ts_nop 0\n\tbuffer_load_dwordx4 %1, %2, %3 offen lds" ::"s"(lds), "v"(voff), "s"(rs), "s"(soff) : "memory");
}
DI void dma_pair(rsrc_t rs, u32 v0, u32 v1, u32 soff, u32 lds) {
    asm volatile("s_mov_b32 m0, %0\n\ts_nop 0\n\t"
                 "buffer_load_dwordx4 %1, %3, %4 offen lds\n\t"
                 "buffer_load_dwordx4 %2, %3, %4 offen offset:1024 lds"
                 ::"s"(lds), "v"(v0), "v"(v1), "s"(rs), "s"(soff) : "memory");
}
struct Pat { u32 v[4]; };
DI Pat mkpat(u32 a, u32 b, u32 step) { Pat p; p.v[0] = a; p.v[1] = b + (step - 1024); p.v[2] = a + 2 * (step - 1024); p.v[3] = b + 3 * (step - 1024); return p; }
DI void dma_unit(rsrc_t rs, const Pat& p, u32 soff, u32 lds) {
    asm volatile("s_mov_b32 m0, %0\n\ts_nop 0\n\t"
                 "buffer_load_dwordx4 %1, %5, %6 offen lds\n\t"
                 "buffer_load_dwordx4 %2, %5, %6 offen offset:1024 lds\n\t"
                 "buffer_load_dwordx4 %3, %5, %6 offen offset:2048 lds\n\t"
                 "buffer_load_dwordx4 %4, %5, %6 offen offset:3072 lds"
                 ::"s"(lds), "v"(p.v[0]), "v"(p.v[1]), "v"(p.v[2]), "v"(p.v[3]), "s"(rs), "s"(soff) : "memory");
}
#define WAITV(n) asm volatile("s_waitcnt vmcnt(" #n ")" ::: "memory")
#define SLEEP(n) asm volatile("s_sleep " #n ::: "memory")
#define BAR() asm volatile("s_waitcnt lgkmcnt(0)\n\ts_barrier" ::: "memory")
#define SB() __builtin_amdgcn_sched_barrier(0)
DI u32x4 ldg128(rsrc_t rs, u32 off) { return __builtin_amdgcn_raw_buffer_load_b128(rs, off, 0, SC1); }
DI u32x2 ldg64(rsrc_t rs, u32 off) { return __builtin_amdgcn_raw_buffer_load_b64(rs, off, 0, SC1); }
DI void stg128(rsrc_t rs, u32 off, u32x4 v) { __builtin_amdgcn_raw_buffer_store_b128(v, rs, off, 0, SC1); }
DI void stg64(rsrc_t rs, u32 off, u32x2 v) { __builtin_amdgcn_raw_buffer_store_b64(v, rs, off, 0, SC1); }
DI u32 san(u32 v) { return v < 0xfffffffeu ? v : 0xfffffffeu; }
DI u32 umax3(u32 a, u32 b, u32 c) { u32 m = a > b ? a : b; return m > c ? m : c; }
DI u32 fmax4(u32 m, u32x4 v) { return umax3(umax3(m, v.x, v.y), v.z, v.w); }
template <int N> DI bool has_empty(const u32x4 (&f)[N]) {
    u32 m = 0;
#pragma unroll
    for (int i = 0; i < N; ++i) m = fmax4(m, f[i]);
    return __any(m == 0xffffffffu);
}
DI f32x2 gelu2(f32x2 y, float k1, float k2, float so) {
    f32x2 t = __builtin_elementwise_fma(y * y, f32x2{k2, k2}, f32x2{k1, k1}) * y;
    f32x2 d = f32x2{__builtin_amdgcn_exp2f(t.x), __builtin_amdgcn_exp2f(t.y)} + 1.f;
    f32x2 r = {__builtin_amdgcn_rcpf(d.x), __builtin_amdgcn_rcpf(d.y)};
    return (y * so) * r;
}
constexpr float GC1 = 2.3022082f, GC2 = 0.10294324f;
DI float f_of(u32 v) { return __builtin_bit_cast(float, v); }
DI u32 u_of(float v) { return __builtin_bit_cast(u32, v); }
DI float xg_sum(float v) {
    auto r = __builtin_amdgcn_permlane32_swap(u_of(v), u_of(v), false, false);
    v = f_of(r[0]) + f_of(r[1]);
    auto s = __builtin_amdgcn_permlane16_swap(u_of(v), u_of(v), false, false);
    return f_of(s[0]) + f_of(s[1]);
}
DI float xg_max(float v) {
    auto r = __builtin_amdgcn_permlane32_swap(u_of(v), u_of(v), false, false);
    v = fmaxf(f_of(r[0]), f_of(r[1]));
    auto s = __builtin_amdgcn_permlane16_swap(u_of(v), u_of(v), false, false);
    return fmaxf(f_of(s[0]), f_of(s[1]));
}
template <int CTRL> DI float dpp(float v) {
    return __builtin_bit_cast(float, __builtin_amdgcn_update_dpp(0, __builtin_bit_cast(int, v), CTRL, 0xf, 0xf, true));
}
DI float row_sum(float v) { v += dpp<0x128>(v); v += dpp<0x124>(v); v += dpp<0x122>(v); v += dpp<0x121>(v); return v; }
DI float row_max(float v) {
    asm volatile("s_nop 1\n\tv_max_f32_dpp %0, %0, %0 row_ror:8 row_mask:0xf bank_mask:0xf\n\ts_nop 1\n\t"
                 "v_max_f32_dpp %0, %0, %0 row_ror:4 row_mask:0xf bank_mask:0xf\n\ts_nop 1\n\t"
                 "v_max_f32_dpp %0, %0, %0 row_ror:2 row_mask:0xf bank_mask:0xf\n\ts_nop 1\n\t"
                 "v_max_f32_dpp %0, %0, %0 row_ror:1 row_mask:0xf bank_mask:0xf" : "+v"(v));
    return v;
}
DI u32x4 swap_units(u32 a0, u32 a1, u32 b0, u32 b1) {
    auto r0 = __builtin_amdgcn_permlane16_swap(a0, b0, false, false);
    auto r1 = __builtin_amdgcn_permlane16_swap(a1, b1, false, false);
    return u32x4{(u32)r0[0], (u32)r1[0], (u32)r0[1], (u32)r1[1]};
}
constexpr float LOG2E = 1.4426950408889634f;
DI float diag4(f32x4 d, u32 c) { float a = (c & 1) ? d[1] : d[0], b = (c & 1) ? d[3] : d[2]; return (c & 2) ? b : a; }


#define PIN(x) asm volatile("" : "+v"(x))
constexpr int NWAVE = 12;
constexpr u32 L_PROG = MISC + 1280, L_HELP = L_PROG + 32, L_AUX = L_PROG + 36, L_LAND = L_PROG + 40, L_TAG = L_PROG + 56, L_CNT = L_PROG + 96;
DI void sigv(u32 addr, u32 val) { asm volatile("ds_write_b32 %0, %1" ::"v"(addr), "v"(val) : "memory"); }
DI void sig(u32 addr, u32 val) { asm volatile("s_mov_b64 exec, 1\n\tds_write_b32 %0, %1\n\ts_mov_b64 exec, -1" ::"v"(addr), "v"(val) : "memory"); }
DI u32 peek(u32 addr) { return *(volatile LAS u32*)addr; }
DI void wait_ge(u32 addr, u32 thr) {
    _Pragma("unroll 1") for (u32 sp = 0; sp < (SPIN_CAP << 4); ++sp) {
        u32 v;
        asm volatile("ds_read_b32 %0, %1\n\ts_waitcnt lgkmcnt(0)" : "=v"(v) : "v"(addr) : "memory");
        if (__all(v >= thr)) break;
        SLEEP(1);
    }
}

DI u32 rl(u32 v, int l) { return __builtin_amdgcn_readlane(v, l); }
DI void snap_wait(u32 aS, u32 thr, u32& sv) {
    _Pragma("unroll 1") for (u32 sp = 0; sp < (SPIN_CAP << 4); ++sp) {
        asm volatile("ds_read_b32 %0, %1\n\ts_waitcnt lgkmcnt(0)" : "=v"(sv) : "v"(aS) : "memory");
        if (__all(sv >= thr)) break;
        SLEEP(1);
    }
}
#define MFMA_ACC(acc, a, b) asm volatile("v_mfma_f32_16x16x32_bf16 %0, %1, %2, %0" : "+v"(acc) : "v"(a), "v"(b))
#define MFMA_NEW(acc, a, b) asm volatile("v_mfma_f32_16x16x32_bf16 %0, %1, %2, 0" : "=&v"(acc) : "v"(a), "v"(b))
#define MFMA_K(first, acc, a, b) do { if (first) MFMA_NEW(acc, a, b); else MFMA_ACC(acc, a, b); } while (0)
#define MFMA_DONE2(x, y) asm volatile("s_nop 7" : "+v"(x), "+v"(y))
template <int K0, bool CS = false>
DI void proj7(u32 ent, const u32 (&tr)[4], const u32x4 (&xf)[14], f32x4 (&acc)[4], u32 aS, u32& sv, u32x4 ones = u32x4{}, f32x4* cs = nullptr) {
    u32x4 f[3][4];
    u32 wa[4];
#pragma unroll
    for (int t = 0; t < 4; ++t) { wa[t] = ent + tr[t]; PIN(wa[t]); }
#pragma unroll
    for (int k = 0; k < 2; ++k)
#pragma unroll
        for (int t = 0; t < 4; ++t) f[k][t] = ldw(wa[t] + k * 4096);
    SB();
#pragma unroll
    for (int k = 0; k < 7; ++k) {
#pragma unroll
        for (int t = 0; t < 4; ++t) {
            MFMA_K(K0 + k == 0, acc[t], f[k % 3][t], xf[K0 + k]);
            if (k < 5) f[(k + 2) % 3][t] = ldw(wa[t] + (k + 2) * 4096);
            if (k == 5 && t == 0) sv = peek(aS);
            if (CS) MFMA_K(K0 + k == 0, cs[t], f[k % 3][t], ones);
            SB();
        }
    }
    asm volatile("s_nop 7" : "+v"(acc[0]), "+v"(acc[1]), "+v"(acc[2]), "+v"(acc[3]));
    if (CS) { PIN(cs[0]); PIN(cs[1]); PIN(cs[2]); PIN(cs[3]); }
}

DI void features(f32x4 a0_, f32x4 a1_, const u32x4* wpf, u32 g, u32x4& pos, u32x4& neg) {
    u32x4 qf = pk8(a0_, a1_);
    if (g == 3) { qf.z = 0; qf.w = 0; }
    f32x4 p0 = mfma(wpf[0], qf, f32x4{}) * LOG2E, p1 = mfma(wpf[1], qf, f32x4{}) * LOG2E;
    const bool v1 = g < 3;
    if (!v1) p1 = p0;
    float mx = fmaxf(fmaxf(fmaxf(p0[0], p0[1]), fmaxf(p0[2], p0[3])), fmaxf(fmaxf(p1[0], p1[1]), fmaxf(p1[2], p1[3])));
    float mn = fminf(fminf(fminf(p0[0], p0[1]), fminf(p0[2], p0[3])), fminf(fminf(p1[0], p1[1]), fminf(p1[2], p1[3])));
    mx = xg_max(mx); mn = -xg_max(-mn);
    const f32x4 d0 = p0 - mx, d1 = p1 - mx, e0 = mn - p0, e1 = mn - p1;
    f32x4 a0, a1, b0, b1;
#pragma unroll
    for (int r = 0; r < 4; ++r) {
        a0[r] = __builtin_amdgcn_exp2f(d0[r]); a1[r] = __builtin_amdgcn_exp2f(d1[r]);
        b0[r] = __builtin_amdgcn_exp2f(e0[r]); b1[r] = __builtin_amdgcn_exp2f(e1[r]);
    }
    if (!v1) { a1 = f32x4{}; b1 = f32x4{}; }
    const f32x4 sa4 = a0 + a1, sb4 = b0 + b1;
    const float ra = __builtin_amdgcn_rcpf(xg_sum((sa4[0] + sa4[1]) + (sa4[2] + sa4[3])));
    const float rb = __builtin_amdgcn_rcpf(xg_sum((sb4[0] + sb4[1]) + (sb4[2] + sb4[3])));
    pos = pk8(a0 * ra, a1 * ra);
    neg = pk8(b0 * rb, b1 * rb);
}

__global__ void __launch_bounds__(NWAVE * 64, 3)
fused_kernel(const u16* __restrict__ x, const u16* __restrict__ wqkv, const u16* __restrict__ wphi, const u16* __restrict__ wo,
             const u16* __restrict__ w1, const u16* __restrict__ w2, const u16* __restrict__ w3, const u16* __restrict__ w4,
             const u16* __restrict__ gw, u16* out, unsigned char* ws) {
    extern __shared__ __attribute__((aligned(16))) unsigned char smem[];
    const u32 tid = threadIdx.x, lane = tid & 63;
    const u32 w = __builtin_amdgcn_readfirstlane(tid >> 6);
    {
        const u32 rt0 = 8 * ((blockIdx.x & 7) >> 1) + (blockIdx.x >> 5);
        const rsrc_t rX = __builtin_amdgcn_make_buffer_rsrc((void*)x, 0, SEQ * DM * 2, 0x00020000);
        u32 l0 = lane; PIN(l0);
        const u32 xa = (rt0 * 64 + (l0 >> 3)) * 896 + (((l0 & 7) ^ (l0 >> 4)) * 16);
        const Pat pX = mkpat(xa, xa ^ 64, 8 * 896);
        dma_unit(rX, pX, (w >> 1) * 128 + (w & 1) * (32 * 896), w * 4096);
        if (w < 2) dma_unit(rX, pX, 6 * 128 + w * (32 * 896), (12 + w) * 4096);
    }
#define TOUCH(rs, off) asm volatile("s_mov_b32 m0, %0\n\ts_nop 0\n\tbuffer_load_dword %1, %2, 0 offen lds" ::"s"(MISC + 1536), "v"(off), "s"(rs) : "memory")
    if (w == 7) {
        u64 pc; int n0;
        asm volatile("s_getpc_b64 %0\n.Lfk_pc:\n\ts_mov_b32 %1, 0\n\ts_max_i32 %1, %1, .Lfk_e0-.Lfk_pc\n\ts_max_i32 %1, %1, .Lfk_e1-.Lfk_pc\n\ts_max_i32 %1, %1, .Lfk_e2-.Lfk_pc"
                     : "=s"(pc), "=s"(n0) :: "scc");
        const rsrc_t rC = __builtin_amdgcn_make_buffer_rsrc((void*)pc, 0, n0, 0x00020000);
        TOUCH(rC, lane < 12 ? ((blockIdx.x >> 3) + lane * 32) * 128 : 0x40000000u);
    }
    const u32 g = lane >> 4, c = lane & 15;
    const u32 b = blockIdx.x;
    const u32 rt = 8 * ((b & 7) >> 1) + (b >> 5), j = 4 * (b & 1) + ((b >> 3) & 3);
    const int row0 = rt * 64;
    const rsrc_t rWS = __builtin_amdgcn_make_buffer_rsrc((void*)ws, 0, WS_END, 0x00020000);
    const bool hasB = j < 7;
    const u32 psnS = hasB ? 0u : 0x40000000u;
    const u32 odd = j & 1;
#define DEF_TR() u32 lt_ = lane; PIN(lt_); const u32 q4 = (lt_ & 15) >> 2, pp = lt_ & 3; \
    const u32 trs = ((q4 >> 1) & 1) | (((lt_ >> 4) & 1) << 1), trb = (8 * (lt_ >> 4) + q4) * 128 + pp * 8
#define TRA(t) (trb + (((t) ^ trs) << 5))
    const u32x4 ONES = {0x3f803f80u, 0x3f803f80u, 0x3f803f80u, 0x3f803f80u};
    const u32x4 EMPTY = {0xffffffffu, 0xffffffffu, 0xffffffffu, 0xffffffffu};
#define LT(n) ((u32)(7 * ((n) + 1)))
    const u32 aC = L_PROG + (lane & 7) * 4;
    if (w == 10) sigv(L_PROG + (lane & 31) * 4, ((lane & 31) >= 16 && (lane & 31) < 24) ? 98 : ((lane & 31) >= 10 && (lane & 31) < 13 ? (lane & 31) + 4 : 0));
    const bool isLoader = w >= 8 && w != 10;
    if (!isLoader) { WAITV(0); BAR(); }

    auto finish_block = [&](u32 p) -> bool {
        const u32 t = p * 64 + lane;
        const u32 off = WS_RS + ((rt * 8 + j) * 8) * 7168 + t * 16;
        u32x4 q[8];
#pragma unroll
        for (int s = 0; s < 8; ++s) q[s] = ldg128(rWS, off + s * 7168);
        const u32 half = t & 1, r = (t >> 1) & 7, nn = t >> 4;
        const u32 row = 8 * j + r, kg = 2 * nn + half;
        const u32x4 x1res = ldg128(rWS, WS_X1 + rt * 57344 + (((row >> 4) * 14 + (kg >> 2)) * 64 + (kg & 3) * 16 + (row & 15)) * 16);
        const float og = 1.f - ldsf(L_GATE + row * 4);
        u32x4 o;
#pragma unroll
        for (int e = 0; e < 4; ++e) {
            f32x2 a = f32x2{blo(x1res[e]), bhi(x1res[e])} * og;
#pragma unroll
            for (int s = 0; s < 8; ++s) a += f32x2{blo(q[s][e]), bhi(q[s][e])};
            o[e] = pk2(a);
        }
        *(GAS u32x4*)((GAS unsigned char*)out + (size_t)(row0 + row) * 896 + nn * 32 + half * 16) = o;
        return true;
    };

    if (isLoader) {
        const rsrc_t rQKV = __builtin_amdgcn_make_buffer_rsrc((void*)wqkv, 0, DM * 3 * DM * 2, 0x00020000);
        const rsrc_t rWO = __builtin_amdgcn_make_buffer_rsrc((void*)wo, 0, DM * DM * 2, 0x00020000);
        const rsrc_t rW1 = __builtin_amdgcn_make_buffer_rsrc((void*)w1, 0, DM * DM * 2, 0x00020000);
        const rsrc_t rW2 = __builtin_amdgcn_make_buffer_rsrc((void*)w2, 0, DM * DM * 2, 0x00020000);
        const rsrc_t rW3 = __builtin_amdgcn_make_buffer_rsrc((void*)w3, 0, DM * FF * 2, 0x00020000);
        const rsrc_t rW4 = __builtin_amdgcn_make_buffer_rsrc((void*)w4, 0, DM * FF * 2, 0x00020000);
        const u32 l3 = w == 11 ? 2 : w - 8;
        u32 myL = L_LAND + l3 * 4, three = 3;
        PIN(myL); PIN(three);
        const u32 r8 = lane >> 3, sl = lane & 7;
        const u32 chA = sl ^ (((r8 >> 1) & 1) << 1), chB = chA ^ 4;
        const u32 jb = hasB ? j : 0;
        u32 rot = l3, pend = 0, seen = 0;
#define LANDED() asm volatile("s_mov_b64 exec, 1\n\tds_add_u32 %0, %1\n\ts_mov_b64 exec, -1" ::"v"(myL), "v"(three) : "memory")
#define POLL() do { u32 st_; asm volatile("s_getreg_b32 %0, hwreg(HW_REG_IB_STS)" : "=s"(st_)); \
            const u32 left_ = (((st_ & 15u) | ((st_ >> 18) & 48u)) + 3) >> 2; \
            _Pragma("unroll 1") while (pend > left_) { LANDED(); --pend; } } while (0)
#define ISSUED() do { ++pend; POLL(); } while (0)
#define FLUSH() do { _Pragma("unroll 1") while (pend > 0) { POLL(); SLEEP(1); } } while (0)
#define ROOM(need) do { if (__builtin_expect(!__all(seen >= (u32)(need)), 0)) { \
            _Pragma("unroll 1") for (u32 sp = 0; sp < (SPIN_CAP << 4); ++sp) { seen = peek(aC); if (__all(seen >= (u32)(need))) break; POLL(); SLEEP(1); } } seen = peek(aC); } while (0)
        u32 met = 0;
#define ROOMS(need) do { const u32 n_ = (need); if (n_ > met) { ROOM(n_ == 5 ? ((lane & 4) ? 0u : 6u) : n_); met = n_; } } while (0)
#define RUN7(k, nB, nA, rs, pat, so0, sstep, ent) do { ROOM((lane & 4) ? (nA) : (nB)); u32 u = rot; \
            _Pragma("unroll 1") for (; u < 7; u += 3) { dma_unit(rs, pat, (so0) + u * (sstep), (ent) + u * 4096); ISSUED(); } rot = u - 7; } while (0)
        const Pat pS = mkpat(r8 * 896 + chA * 16, r8 * 896 + chB * 16, 8 * 896);
        const Pat pQ = mkpat(r8 * 2688 + (chA >> 2) * 56 + (chA & 3) * 16, r8 * 2688 + (chB >> 2) * 56 + (chB & 3) * 16, 8 * 2688);
#define LD_STD(k, nB, nA, rs, half, ent) RUN7(k, nB, nA, rs, pS, (224 * (half)) * 896 + jb * 128, 32 * 896, ent)
#define LD_QKV(k, nB, nA, sec, half, ent) RUN7(k, nB, nA, rQKV, pQ, (224 * (half)) * 2688 + (sec) * 896 + 112 * j, 32 * 2688, ent)
        {
            u32 u = rot;
            dma_unit(rW1, pS, jb * 128 + u * (32 * 896), E2 + u * 4096); u += 3;
            dma_unit(rW1, pS, jb * 128 + u * (32 * 896), E2 + u * 4096); u += 3;
            WAITV(8);
            BAR();
            pend = 2;
#pragma unroll 1
            for (; u < 14; u += 3) { dma_unit(rW1, pS, jb * 128 + u * (32 * 896), E2 + u * 4096); ISSUED(); }
            rot = u - 14;
        }
        LD_QKV(3, 0, 0, 1, 0, E4);
        LD_QKV(4, 1, 1, 1, 1, E0);
        LD_QKV(5, 1, 1, 0, 0, E1);
        LD_QKV(6, 2, 2, 0, 1, E2);
        LD_STD(7, 3, 2, rW2, 0, E3);
        LD_STD(8, 0, 3, rW2, 1, E4);
        LD_QKV(9, 0, 4, 2, 0, E0);
        LD_QKV(10, 4, 0, 2, 1, E1);
        const Pat pT = mkpat(r8 * 3584 + chA * 16, r8 * 3584 + chB * 16, 8 * 3584);
        const Pat pU = mkpat((r8 + (chA >> 2) * 32) * 3584 + (chA & 3) * 16, (r8 + (chB >> 2) * 32) * 3584 + (chB & 3) * 16, 8 * 3584);
        const u32 s3f = 448 * j + odd * 64, s3h = 448 * j + (odd ? 0 : 384);
        const u32 pr = 16 * (r8 >> 2) + (r8 & 3);
        const Pat pP = mkpat(pr * 896 + chA * 16, pr * 896 + chB * 16, 4 * 896);
        const u32 s4f = (224 * j + odd * 32) * 896, s4l = (224 * j + (odd ? 0 : 192)) * 896, sWo = 56 * j * 896;
        {
            u64 tt = 0x1065a98765a987ull, tn = 0xc9555555110000ull, ts = 0x7060543201ull;
            u32 so = s3f;
#pragma unroll 1
            for (u32 ks = 0; ks < 14; ++ks) {
                ROOMS(5 + ((u32)tn & 15u));
                dma_unit(rW3, pT, so + rot * 128, ((u32)tt & 15u) * 12288 + rot * 4096); ++pend;
                if (ks % 3 == 2) POLL();
                const u32 si = (u32)ts & 15u;
                if (si) {
                    if (rot == 0) {
                        ROOMS(5 + ((0x5510000u >> (4 * (si - 1))) & 15u));
                        dma_unit(rW3, pU, s3h + (si - 1) * (64 * 3584), (u32)(0x22210e24232221ull >> (8 * (si - 1)) & 255u) * 4096); ++pend; rot = 2;
                    } else --rot;
                }
                tt >>= 4; tn >>= 4; ts >>= 4; so += 32 * 3584;
            }
        }
        {
            u64 tt = 0x1870365a2987ull, tn = 0xb99655432210ull;
            const u32 o0 = s4f + rot * (32 * 896), o2 = rot == 0 ? s4l : sWo + (rot - 1) * (28 * 896);
#pragma unroll 1
            for (u32 cb = 0; cb < 7; ++cb) {
                if (cb == 4) { tt = 0x365a29b4ull; tn = 0xedddcccbbull; }
#pragma unroll
                for (u32 t3 = 0; t3 < 3; ++t3) {
                    ROOMS(18 + ((u32)tn & 15u));
                    const u32 ld = ((u32)tt & 15u) * 12288 + rot * 4096;
                    if (t3 < 2) dma_unit(rW4, pP, o0 + t3 * (96 * 896) + cb * 128, ld);
                    else if (rot == 0) dma_unit(rW4, pP, o2 + cb * 128, ld);
                    else dma_unit(rWO, pP, o2 + cb * 128, ld);
                    ISSUED();
                    tt >>= 4; tn >>= 4;
                }
            }
        }
        FLUSH();
        wait_ge(L_HELP, 102);
        finish_block(l3);
        asm volatile(".Lfk_e0:");
    } else if (w == 10) {
        const rsrc_t rPHI = __builtin_amdgcn_make_buffer_rsrc((void*)wphi, 0, 16 * 28 * 28 * 2, 0x00020000);
        const rsrc_t rGW = __builtin_amdgcn_make_buffer_rsrc((void*)gw, 0, DM * 2, 0x00020000);
        const u32 head = 2 * j + (rt >> 4), chunk = rt & 15;
        {
            const u32 o_ = lane < 12 ? WS_MB1 + (lane >> 2) * 131072 + (rt * 8 + j) * 512 + (lane & 3) * 128 : 0x40000000u;
            asm volatile("s_mov_b32 m0, %0\n\ts_nop 0\n\tbuffer_load_dword %1, %2, 0 offen sc1 lds" ::"s"(MISC + 1536), "v"(o_), "s"(rWS) : "memory");
        }
        dma16(rGW, lane * 16, 0, GWL);
        dma_pair(rPHI, lane * 16, lane * 16, j * 3136, WPHI);
        dma_pair(rPHI, lane * 16, lane * 16, j * 3136 + 2048, WPHI + 2048);
        const u32 tag = __builtin_amdgcn_readfirstlane(__builtin_amdgcn_raw_buffer_load_b32(rWS, WS_EPOCH, 0, SC1)) + 1;
        const u32 taghi = tag * 0x9E3779B1u ^ 0x5bd1e995u;
        WAITV(0);
        sig(L_TAG, tag);
        sig(L_HELP, 1);
        const u32 me = rt * 8 + j;
#define PD0 28
#define PD1 8
#define POLL_ALL(off, first, gap) do { const u32 o_ = (off); SLEEP(first); _Pragma("unroll 1") for (u32 sp = 0; sp < SPIN_CAP; ++sp) { \
            u32x2 fl = ldg64(rWS, o_); if (__all(fl.x == tag && fl.y == taghi)) break; SLEEP(gap); } } while (0)
#define POLL_ALL2(off, first, gap) POLL_ALL(off, first, gap)
        {
            const int rw = (int)rt - 1 + (int)(lane >> 3);
            wait_ge(L_CNT, 4);
            POLL_ALL2(WS_MB1 + me * 512 + ((lane < 24 && (u32)rw < 32u) ? lane : 8 + (lane & 7)) * 8, PD0, PD1);
            sig(L_HELP, 100);
            wait_ge(L_CNT + 4, 4);
            POLL_ALL2(WS_MB1 + me * 512 + 256 + (lane & 7) * 8, PD0, PD1);
            sig(L_AUX, 100);
            POLL_ALL2(WS_MB3 + me * 512 + (lane & 31) * 8, 0, 12);
        }
        u32x4 pv[8];
        {
            const u32 poff = WS_KVP + (head * 32 + g) * 4096 + chunk * 256 + c * 16;
#pragma unroll
            for (int i = 0; i < 8; ++i) pv[i] = ldg128(rWS, poff + i * 16384);
            u32x4 o;
#pragma unroll
            for (int e = 0; e < 4; ++e) {
                f32x2 s = {0.f, 0.f};
#pragma unroll
                for (int i = 0; i < 8; ++i) s += f32x2{blo(pv[i][e]), bhi(pv[i][e])};
                o[e] = pk(xg_sum(s.x), xg_sum(s.y));
            }
            if (lane < 16) stg128(rWS, WS_KVF + head * 4096 + chunk * 256 + lane * 16, o);
            WAITV(0);
            if (lane < 32) stg64(rWS, WS_MB2 + (lane * 8 + j) * 512 + rt * 8, u32x2{tag, taghi});
        }
        POLL_ALL2(WS_MB2 + me * 512 + (lane & 31) * 8, PD0, 8);
#pragma unroll
        for (int i = 0; i < 8; ++i) pv[i] = ldg128(rWS, WS_KVF + 2 * j * 4096 + lane * 16 + i * 1024);
        wait_ge(aC, (lane & 4) ? 5 : 7);
#pragma unroll
        for (int i = 0; i < 8; ++i) sts128(MR + i * 1024 + lane * 16, pv[i]);
        sig(L_HELP, 101);
        wait_ge(L_CNT + 16, 8);
        POLL_ALL2(WS_MB2 + me * 512 + 384 + (lane & 7) * 8, 12, 2);
        sig(L_HELP, 102);
        wait_ge(L_CNT + 12, 8);
        POLL_ALL2(WS_MB2 + me * 512 + 256 + (lane & 7) * 8, 8, 1);
        sig(L_HELP, 103);
        asm volatile(".Lfk_e1:");
        if (b == 0 && lane == 0) __builtin_amdgcn_raw_buffer_store_b32(tag, rWS, WS_EPOCH, 0, SC1);
    } else {
        const u32 mt = w & 3, hf = w >> 2;
        const u32 myrow = 16 * mt + c;
        DEF_TR();
        const u32 myP = L_PROG + w * 4;
#define SIGP(n) sig(myP, (n))
        const u32 aS = L_PROG + (c < 13 ? c : 0) * 4;
        u32 sv = 0;
#define NEEDL(n, id) do { if (__builtin_expect(!__all(c < 10 || c > 12 || sv >= LT(n)), 0)) { snap_wait(aS, (c >= 10 && c <= 12) ? LT(n) : 0u, sv); } } while (0)
#define NEEDW(wd, n, id) do { if (__builtin_expect(rl(sv, wd) < (u32)(n), 0)) { snap_wait(aS, c == (wd) ? (u32)(n) : 0u, sv); } } while (0)
#define NEEDP(n, id) do { if (__builtin_expect(!__all(c >= 8 || sv >= (u32)(n)), 0)) { snap_wait(aS, c < 8 ? (u32)(n) : 0u, sv); } } while (0)
#define PUBLISH(word, n, off) do { WAITV(0); PUBLISH_(word, n, off); } while (0)
#define PUBLISH_(word, n, off) do { u32 o_; \
            asm volatile("s_mov_b64 exec, 1\n\tds_add_rtn_u32 %0, %1, %2\n\ts_mov_b64 exec, -1\n\ts_waitcnt lgkmcnt(0)" : "=&v"(o_) : "v"(L_CNT + (word) * 4), "v"(1u) : "memory"); \
            if (__builtin_amdgcn_readfirstlane(o_) == (n) - 1) { const u32 tg_ = peek(L_TAG); stg64(rWS, (off), u32x2{tg_, tg_ * 0x9E3779B1u ^ 0x5bd1e995u}); } } while (0)
#define NEEDT(nB, nA, id) do { const u32 t_ = c < 4 ? (u32)(nB) : (c < 8 ? (u32)(nA) : 0u); if (__builtin_expect(!__all(sv >= t_), 0)) { snap_wait(aS, t_, sv); } } while (0)
        const u32 tr[4] = {TRA(0), TRA(1), TRA(2), TRA(3)};
        u32 hoff[2], x1o[2], zoff[2];
#pragma unroll
        for (int p2 = 0; p2 < 2; ++p2) {
            const u32 nt = 2 * p2 + (g & 1);
            const u32 grp = (4 * j + nt) / 7;
            const int dlt = grp == 0 ? -1 : (grp == 1 ? 1 : (grp == 2 ? -2 : 2));
            const u32 uu = 2 * nt + (g >> 1);
            const u32 cpart = (((2 * j + (uu >> 2)) * 64 + (uu & 3) * 16) * 16) | psnS;
            const int R = row0 + myrow, Rd = R - dlt;
            hoff[p2] = (u32)Rd < (u32)SEQ ? WS_H + cpart + (Rd >> 4) * (14 * 1024) + (Rd & 15) * 16 : 0xfffffff0u;
            const u32 own = cpart + (R >> 4) * (14 * 1024) + (R & 15) * 16;
            x1o[p2] = WS_X1 + own;
            zoff[p2] = (u32)(R + dlt) < (u32)SEQ ? 0xfffffff0u : WS_H + own;
        }
        if (w == 6) {
        {
            const rsrc_t rQKV = __builtin_amdgcn_make_buffer_rsrc((void*)wqkv, 0, DM * 3 * DM * 2, 0x00020000);
            const rsrc_t rWO = __builtin_amdgcn_make_buffer_rsrc((void*)wo, 0, DM * DM * 2, 0x00020000);
            const rsrc_t rW2 = __builtin_amdgcn_make_buffer_rsrc((void*)w2, 0, DM * DM * 2, 0x00020000);
            const rsrc_t rW3 = __builtin_amdgcn_make_buffer_rsrc((void*)w3, 0, DM * FF * 2, 0x00020000);
            const rsrc_t rW4 = __builtin_amdgcn_make_buffer_rsrc((void*)w4, 0, DM * FF * 2, 0x00020000);
            TOUCH(rQKV, lane < 37 ? (b * 37 + lane) * 128 : 0x40000000u);
            TOUCH(rW2, lane < 13 ? (b * 13 + lane) * 128 : 0x40000000u);
            TOUCH(rW3, lane < 49 ? (b * 49 + lane) * 128 : 0x40000000u);
            TOUCH(rW4, lane < 49 ? (b * 49 + lane) * 128 : 0x40000000u);
            TOUCH(rWO, lane < 13 ? (b * 13 + lane) * 128 : 0x40000000u);
        }
        }

#define LOAD_XF() u32x4 xf[14]; { const u32 sx = (myrow >> 1) & 7, a0 = myrow * 128 + ((g ^ sx) * 16), a1 = a0 ^ 64; \
            _Pragma("unroll") for (int ks = 0; ks < 14; ++ks) xf[ks] = lds128((ks >> 1) * XT_CB + ((ks & 1) ? a1 : a0)); }
#define LOAD_WPF() u32x4 wpf[4]; NEEDW(8, 1, 30); _Pragma("unroll") for (int q = 0; q < 4; ++q) { \
            const u32 a = WPHI + (q >> 1) * 1568 + (4 * g + q4) * 56 + (q & 1) * 32 + pp * 8; u32x4 t = ldw2(a, a + 16 * 56); if (g == 3) { t.z = 0; t.w = 0; } wpf[q] = t; }
        constexpr u32 G_OFF[14] = {86016, 98304, 110592, 122880, 61440, 73728, 86016, 98304, 110592, 122880, 61440, 73728, 0, 12288};
        constexpr u32 S_OFF[7] = {135168, 139264, 143360, 147456, 57344, 135168, 139264};
        constexpr u32 G_LAND[14] = {87, 91, 94, 98, 102, 106, 110, 113, 117, 120, 124, 127, 130, 133};
        constexpr u32 S_LAND[7] = {88, 95, 99, 103, 107, 114, 121};
        constexpr u32 H_OFF[21] = {86016, 98304, 110592, 24576, 122880, 61440, 73728, 36864, 0, 86016, 98304, 12288, 49152, 135168, 110592, 24576, 122880, 61440, 73728, 36864, 0};
        u32x4 phq[2][2];
        if (hf == 0) {
            if (rt == 0 || rt == 31) { stg128(rWS, zoff[0], u32x4{0, 0, 0, 0}); stg128(rWS, zoff[1], u32x4{0, 0, 0, 0}); }
            LOAD_XF();
            u32x2 xres[4];
            {
                const u32 sx = (myrow >> 1) & 7;
#pragma unroll
                for (int t = 0; t < 4; ++t) xres[t] = lds64((hasB ? j : 0) * XT_CB + myrow * 128 + (((2 * t + (g >> 1)) ^ sx) * 16) + (g & 1) * 8);
            }
            SIGP(1);
            {
                u32x4 on1 = ONES; PIN(on1);
                f32x4 s0, s1;
#pragma unroll
                for (int ks = 0; ks < 14; ++ks) { MFMA_K(ks == 0, s0, on1, xf[ks]); MFMA_K(ks == 0, s1, xf[ks], xf[ks]); }
                MFMA_DONE2(s0, s1);
                const float mean = s0[0] * (1.f / DM);
                const float var = fmaxf(xg_sum(g == (c >> 2) ? diag4(s1, c) : 0.f) * (1.f / DM) - mean * mean, 0.f);
                const float rstd = __builtin_amdgcn_rsqf(var + 1e-5f);
                const float k1 = -GC1 * rstd, k2 = -GC2 * rstd * rstd * rstd;
                f32x4 a1c[4];
                NEEDL(2, 2);
                proj7<0>(E2, tr, xf, a1c, aS, sv);
                SIGP(2);
                NEEDL(3, 3);
                proj7<7>(E3, tr, xf, a1c, aS, sv);
                SIGP(3);
                NEEDT(0, 2, 4);
                u32 pkd[4][2];
#pragma unroll
                for (int t = 0; t < 4; ++t) {
                    const f32x4 cs = __builtin_bit_cast(f32x4, lds128(L_CS1 + (16 * t + 4 * g) * 4));
                    f32x2 y0 = f32x2{a1c[t][0], a1c[t][1]} - mean * f32x2{cs[0], cs[1]}, y1 = f32x2{a1c[t][2], a1c[t][3]} - mean * f32x2{cs[2], cs[3]};
                    pkd[t][0] = (pk2(gelu2(y0, k1, k2, rstd))); pkd[t][1] = (pk2(gelu2(y1, k1, k2, rstd)));
                    if (t & 1) stg128(rWS, hoff[t >> 1], swap_units(pkd[t - 1][0], pkd[t - 1][1], pkd[t][0], pkd[t][1]));
                }
            }
            {
                NEEDW(8, 1, 30);
                const int rc = (int)rt - 1 + (int)(lane >> 3);
                PUBLISH(0, 4, (lane < 24 && (u32)rc < 32u) ? WS_MB1 + (rc * 8 + (lane & 7)) * 512 + ((2 - (lane >> 3)) * 8 + j) * 8 : 0xfffffff0u);
            }
            f32x4 aq[4];
            NEEDL(6, 5);
            proj7<0>(E1, tr, xf, aq, aS, sv);
            SIGP(4);
            NEEDL(7, 6);
            proj7<7>(E2, tr, xf, aq, aS, sv);
            SIGP(5);
            u32x4 hfr[14];
            const u32 goff = (WS_H + rt * 57344 + (mt * 14 * 64 + lane) * 16) | psnS;
            NEEDW(8, 100, 7);
#pragma unroll
            for (int i = 0; i < 14; ++i) hfr[i] = ldg128(rWS, goff + i * 1024);
            f32x4 a2[4];
            NEEDL(9, 8);
            proj7<0>(E3, tr, hfr, a2, aS, sv);
            proj7<7>(E4, tr, hfr, a2, aS, sv);
            u32 pkd[4][2];
#pragma unroll
            for (int t = 0; t < 4; ++t) {
                pkd[t][0] = (pk(a2[t][0] + blo(xres[t].x), a2[t][1] + bhi(xres[t].x)));
                pkd[t][1] = (pk(a2[t][2] + blo(xres[t].y), a2[t][3] + bhi(xres[t].y)));
                if (t & 1) stg128(rWS, x1o[0] + (t >> 1) * 1024, swap_units(pkd[t - 1][0], pkd[t - 1][1], pkd[t][0], pkd[t][1]));
            }
            SIGP(6);
            PUBLISH(1, 4, lane < 8 ? WS_MB1 + (rt * 8 + lane) * 512 + 256 + j * 8 : 0xfffffff0u);
            {
                LOAD_WPF();
                SIGP(7);
                features(aq[0], aq[1], wpf, g, phq[0][0], phq[0][1]);
                features(aq[2], aq[3], wpf + 2, g, phq[1][0], phq[1][1]);
                PIN(phq[0][0]); PIN(phq[0][1]); PIN(phq[1][0]); PIN(phq[1][1]);
            }
        } else {
            LOAD_XF();
            SIGP(1);
            {
                u32 ca = E2 + TRA(mt); PIN(ca);
                f32x4 cs = {};
                NEEDL(3, 2);
#pragma unroll
                for (int k = 0; k < 14; ++k) cs = mfma(ldw(ca + k * 4096), ONES, cs);
                if (c == 0) sts128(L_CS1 + (16 * mt + 4 * g) * 4, __builtin_bit_cast(u32x4, cs));
                SIGP(2);
            }
            {
                NEEDW(8, 1, 30);
                f32x4 s0 = {};
#pragma unroll
                for (int ks = 0; ks < 14; ++ks) s0 = mfma(lds128(GWL + ks * 64 + g * 16), xf[ks], s0);
                if (g == 0) stsf(L_GATE + myrow * 4, __builtin_amdgcn_rcpf(1.f + __builtin_amdgcn_exp2f(-LOG2E * s0[0])));
            }
            const u32 wsw = ((c >> 1) & 1) | (((c >> 3) & 1) << 1);
            const u32 wrb = E2 + myrow * 128 + g * 8;
            {
                f32x4 ak[4];
                NEEDL(4, 5);
                proj7<0>(E4, tr, xf, ak, aS, sv);
                SIGP(3);
                NEEDL(5, 6);
                proj7<7>(E0, tr, xf, ak, aS, sv);
                SIGP(4);
                LOAD_WPF();
                u32x4 pos[2], neg[2];
                features(ak[0], ak[1], wpf, g, pos[0], neg[0]);
                features(ak[2], ak[3], wpf + 2, g, pos[1], neg[1]);
                NEEDT(5, 0, 32);
#pragma unroll
                for (int hd = 0; hd < 2; ++hd) {
                    *(LAS u32x2*)(wrb + hd * 8192 + ((0 ^ wsw) << 5)) = u32x2{pos[hd].x, pos[hd].y};
                    *(LAS u32x2*)(wrb + hd * 8192 + ((1 ^ wsw) << 5)) = u32x2{pos[hd].z, pos[hd].w};
                    *(LAS u32x2*)(wrb + hd * 8192 + ((2 ^ wsw) << 5)) = u32x2{neg[hd].x, neg[hd].y};
                    *(LAS u32x2*)(wrb + hd * 8192 + ((3 ^ wsw) << 5)) = u32x2{neg[hd].z, neg[hd].w};
                }
            }
            {
                f32x4 av[4];
                NEEDL(10, 10);
                proj7<0>(E0, tr, xf, av, aS, sv);
                NEEDL(11, 11);
                proj7<7>(E1, tr, xf, av, aS, sv);
#pragma unroll
                for (int t = 0; t < 4; ++t) {
                    u32x2 vv = {pk(av[t][0], av[t][1]), pk(av[t][2], av[t][3])};
                    if ((t & 1) && g == 3) vv = u32x2{0x00003f80u, 0u};
                    *(LAS u32x2*)(wrb + 16384 + ((t ^ wsw) << 5)) = vv;
                }
            }
            SIGP(5);
            NEEDT(0, 5, 13);
            {
                const u32 pa = E2 + TRA(2 * (mt >> 1)), pb = E2 + TRA(2 * (mt >> 1) + 1);
                u32x4 kp[2];
#pragma unroll
                for (int hd = 0; hd < 2; ++hd) {
                    f32x4 kv[2] = {};
                    const u32 va = E2 + 16384 + TRA(2 * hd + (mt & 1));
#pragma unroll
                    for (int ks = 0; ks < 2; ++ks) {
                        const u32x4 vf = ldw(va + ks * 4096);
                        kv[0] = mfma(ldw(pa + hd * 8192 + ks * 4096), vf, kv[0]);
                        kv[1] = mfma(ldw(pb + hd * 8192 + ks * 4096), vf, kv[1]);
                    }
                    const u32x4 t = pk8(kv[0], kv[1]);
                    kp[hd] = t;
                }
                SIGP(6);
#pragma unroll
                for (int hd = 0; hd < 2; ++hd) stg128(rWS, WS_KVP + ((2 * j + hd) * 32 + rt) * 4096 + mt * 1024 + lane * 16, kp[hd]);
                PUBLISH(2, 4, lane < 32 ? WS_MB3 + (lane * 8 + j) * 512 + rt * 8 : 0xfffffff0u);
            }
        }
        u32x4 pre[6][2];
        {
            u32x4 xv[7];
            const u32 x1off = WS_X1 + rt * 57344 + ((mt * 14 + 7 * hf) * 64 + lane) * 16;
            NEEDW(9, 100, 12);
#pragma unroll
            for (int i = 0; i < 7; ++i) xv[i] = ldg128(rWS, x1off + i * 1024);
            NEEDT(0, 5, 15);
#pragma unroll
            for (int i = 0; i < 7; ++i) sts128(((7 * hf * 4 + mt) * 64 + lane) * 16 + i * 4096, xv[i]);
        }
        {
            { u32 l_ = lane; PIN(l_); wait_ge(L_LAND + ((l_ & 3) == 3 ? 0 : (l_ & 3)) * 4, G_LAND[5]); }
            const bool w6 = w == 6;
            const u32 ub = w6 ? 0 : ((w >> 1) % 3) * 4096;
            const u32 e0 = ub + TRA(2 * (w & 1)), e1 = ub + TRA(2 * (w & 1) + 1), o0 = w6 ? TRA(2) : e0, o1 = w6 ? TRA(3) : e1;
#pragma unroll
            for (int ks = 0; ks < 6; ++ks) {
                const u32 off = w6 ? S_OFF[ks >> 1] : G_OFF[ks];
                pre[ks][0] = ldw(((ks & 1) ? o0 : e0) + off); pre[ks][1] = ldw(((ks & 1) ? o1 : e1) + off);
            }
        }
        SIGP(11);
        u32 ln = lane; PIN(ln);
        const u32 g2 = ln >> 4, c2 = ln & 15;
        const u32 trs2 = ((ln >> 3) & 1) | ((g2 & 1) << 1);
        const u32 trb2 = (8 * g2 + (c2 >> 2)) * 128 + (ln & 3) * 8;
#define TRB(t) (trb2 + (((t) ^ trs2) << 5))
        const u32 aC2 = L_PROG + (ln & 7) * 4, aL2 = L_LAND + (c2 < 3 ? c2 : 0) * 4;
        const u32 mh4 = w & 1, nt4 = w >> 1;
        u32 rsoff;
        {
            const u32 row = 16 * (2 * mh4 + (g2 & 1)) + c2;
            rsoff = WS_RS + ((rt * 8 + (row >> 3)) * 8 + j) * 7168 + (nt4 * 8 + (row & 7)) * 32 + (g2 >> 1) * 16;
        }
        constexpr u32 B_LAND[7] = {142, 151, 160, 169, 178, 187, 196};
        constexpr u32 AF_OFF[3] = {12288, 49152, 135168};

        const u32 xb = ln * 16;
        u32 wb4[3];
        wb4[0] = TRB(nt4); wb4[1] = wb4[0] + 61440; wb4[2] = wb4[0] + 122880;
        #define W4_OFF(cb, k) (H_OFF[3 * (cb) + (k) / 3] + ((k) % 3) * 4096)
#define W4_RD(cb, k) ldw(wb4[W4_OFF(cb, k) / 61440] + W4_OFF(cb, k) % 61440)
        u32x4 f4[2][7];
#define S4_PRE() do { wait_ge(aL2, B_LAND[0]); _Pragma("unroll") for (int k = 0; k < 7; ++k) f4[0][k] = W4_RD(0, k); SIGP(27); } while (0)
        if (w < 7) {
            f32x4 a3[4][2], a3s[2];
            u32x4 f[2][2], xr[2][4], on3 = ONES;
            u32 pa = ln == 0 ? myP : (ln == 1 ? myP + 64 : MISC + 1536 + ln * 4), two = ln == 1 ? 8 : 2;
            const u32 aQ = L_PROG + (c2 < 7 ? 16 + c2 : (c2 < 10 ? c2 + 3 : 10)) * 4;
            PIN(on3); PIN(pa); PIN(two);
#define S3_F(ks, t) ((ks) < 6 ? pre[(ks) < 6 ? (ks) : 0][t] : f[(ks) & 1][t])
#define S3_RX(ks, m) xr[(ks) & 1][m] = lds128(xb + ((ks) * 4 + (m)) * 1024)
#define S3_LOOP(RW, LAND) do { \
            wait_ge(aC2, 11); \
            u32 lv = 0; \
            S3_RX(0, 0); S3_RX(0, 1); S3_RX(0, 2); S3_RX(0, 3); \
            SB(); \
            _Pragma("unroll") for (int ks = 0; ks < 14; ++ks) { \
                const int kn = ks < 13 ? ks + 1 : 13; \
                if ((ks & 1) && ks < 13) { if (__builtin_expect(!__all(lv >= (u32)(98 + 4 * (ks - 1))), 0)) { wait_ge(aQ, 98 + 4 * (ks - 1)); } } \
                MFMA_K(ks == 0, a3s[0], S3_F(ks, 0), on3); \
                if (ks < 13 && kn >= 6) RW(kn, 0); \
                SB(); \
                MFMA_K(ks == 0, a3s[1], S3_F(ks, 1), on3); \
                if (ks < 13 && kn >= 6) RW(kn, 1); \
                SB(); \
                _Pragma("unroll") for (int m = 0; m < 4; ++m) { \
                    MFMA_K(ks == 0, a3[m][0], S3_F(ks, 0), xr[ks & 1][m]); \
                    if (ks < 13) S3_RX(kn, m); \
                    SB(); \
                    MFMA_K(ks == 0, a3[m][1], S3_F(ks, 1), xr[ks & 1][m]); \
                    if (m == 1 && !(ks & 1)) lv = peek(aQ); \
                    if (m == 3 && !(ks & 1)) asm volatile("ds_add_u32 %0, %1" ::"v"(pa), "v"(two) : "memory"); \
                    SB(); \
                } \
            } } while (0)
            {
                const bool w6 = w == 6;
                u32 wa[2][2];
                wa[0][0] = (w6 ? 0 : (w >> 1) * 4096) + TRB(2 * (w & 1)); wa[0][1] = (w6 ? 0 : (w >> 1) * 4096) + TRB(2 * (w & 1) + 1);
                wa[1][0] = w6 ? TRB(2) : wa[0][0]; wa[1][1] = w6 ? TRB(3) : wa[0][1];
                PIN(wa[0][0]); PIN(wa[0][1]); PIN(wa[1][0]); PIN(wa[1][1]);
#define S3_RWG(ks, t) do { u32 a_ = wa[(ks) & 1][t] + (w6 ? S_OFF[(ks) >> 1] : G_OFF[ks]); PIN(a_); f[(ks) & 1][t] = ldw(a_); } while (0)
                S3_LOOP(S3_RWG, 0);
            }
            asm volatile("s_nop 7" ::: "memory");
            SIGP(26);
            PIN(wb4[0]); PIN(wb4[1]); PIN(wb4[2]);
            S4_PRE();
            wait_ge(aC2, 26);
            float mean[4], k1[4], k2[4], so[4];
#pragma unroll
            for (int m = 0; m < 4; ++m) {
                const u32 row = 16 * m + c2;
                mean[m] = ldsf(L_SUM1 + row * 4) * (1.f / DM);
                const float var = fmaxf(ldsf(L_SQ1 + row * 4) * (1.f / DM) - mean[m] * mean[m], 0.f);
                const float rstd = __builtin_amdgcn_rsqf(var + 1e-5f);
                k1[m] = -GC1 * rstd; k2[m] = -GC2 * rstd * rstd * rstd; so[m] = rstd * (1.f - ldsf(L_GATE + row * 4));
            }
            const u32 hdst = (w < 3 ? AF_OFF[0] : (w < 6 ? AF_OFF[1] : AF_OFF[2])) + (w % 3) * 4096 + ln * 16;
#pragma unroll
            for (int m = 0; m < 4; ++m) {
                u32x4 o;
#pragma unroll
                for (int t = 0; t < 2; ++t) {
                    f32x2 y0 = f32x2{a3[m][t][0], a3[m][t][1]} - mean[m] * f32x2{a3s[t][0], a3s[t][1]};
                    f32x2 y1 = f32x2{a3[m][t][2], a3[m][t][3]} - mean[m] * f32x2{a3s[t][2], a3s[t][3]};
                    o[2 * t] = pk2(gelu2(y0, k1[m], k2[m], so[m])); o[2 * t + 1] = pk2(gelu2(y1, k1[m], k2[m], so[m]));
                }
                sts128(hdst + m * 1024, o);
            }
        } else {
            f32x4 s1[4], s2[4];
            u32x4 v[2][4], on7 = ONES;
            PIN(on7);
            wait_ge(aC2, 11);
#pragma unroll
            for (int q = 0; q < 4; ++q) v[0][q] = lds128(xb + q * 1024);
            SB();
#pragma unroll
            for (int ks = 0; ks < 14; ++ks) {
#pragma unroll
                for (int q = 0; q < 4; ++q) {
                    MFMA_K(ks == 0, s1[q], on7, v[ks & 1][q]);
                    if (ks < 13) v[(ks + 1) & 1][q] = lds128(xb + ((ks + 1) * 4 + q) * 1024);
                    SB();
                    MFMA_K(ks == 0, s2[q], v[ks & 1][q], v[ks & 1][q]);
                }
                if (!(ks & 1)) SIGP(13 + ks);
            }
            MFMA_DONE2(s1[3], s2[3]);
            if (g2 == (c2 >> 2)) {
#pragma unroll
                for (int m = 0; m < 4; ++m) { stsf(L_SUM1 + (16 * m + c2) * 4, s1[m][0]); stsf(L_SQ1 + (16 * m + c2) * 4, diag4(s2[m], c2)); }
            }
            SIGP(26);
            PIN(wb4[0]); PIN(wb4[1]); PIN(wb4[2]);
            S4_PRE();
            wait_ge(aC2, 26);
        }
        u32x4 af[2][9];
        if (hf == 0) {
            wait_ge(L_HELP, 101);
            const float gt = ldsf(L_GATE + (16 * mt + c2) * 4);
#pragma unroll
            for (int hd = 0; hd < 2; ++hd) {
                u32x4 kf[4];
#pragma unroll
                for (int i = 0; i < 4; ++i) kf[i] = lds128(MR + (hd * 4 + i) * 1024 + ln * 16);
                f32x4 o0 = mfma(kf[0], phq[hd][0], f32x4{}); o0 = mfma(kf[2], phq[hd][1], o0);
                f32x4 o1 = mfma(kf[1], phq[hd][0], f32x4{}); o1 = mfma(kf[3], phq[hd][1], o1);
                const float den = xg_sum(g2 == 3 ? o1[0] : 0.f) + 1e-6f;
                const float sc = gt * __builtin_amdgcn_rcpf(den);
                u32x4 f = pk8(o0 * sc, o1 * sc);
                if (g2 == 3) { f.z = 0; f.w = 0; }
                sts128(AF_OFF[2] + (1 + hd) * 4096 + mt * 1024 + ln * 16, f);
            }
        }
        SIGP(28);
        {
            wait_ge(aC2, 28);
            {
                u32 ab = (2 * mh4) * 1024 + ln * 16, ab2 = ab + AF_OFF[2];
                PIN(ab); PIN(ab2);
#pragma unroll
                for (int k = 0; k < 9; ++k)
#pragma unroll
                    for (int m = 0; m < 2; ++m) af[m][k] = lds128((k < 6 ? ab + AF_OFF[k / 3] : ab2) + (k % 3) * 4096 + m * 1024);
            }
            SIGP(29);
            u32 lv = peek(aL2);
#pragma unroll
            for (int cb = 1; cb < 8; ++cb) {
                const int p = cb - 1;
                if (cb < 7) { if (__builtin_expect(!__all(lv >= B_LAND[cb < 7 ? cb : 0]), 0)) { wait_ge(aL2, B_LAND[cb < 7 ? cb : 0]); } }
                const u32x4 wo0 = W4_RD(p, 7), wo1 = W4_RD(p, 8);
                f32x4 a4[2];
#pragma unroll
                for (int k = 0; k < 7; ++k) {
                    if (k == 0) asm volatile("v_mfma_f32_16x16x32_bf16 %0, %1, %2, 0" : "=&v"(a4[0]) : "v"(f4[p & 1][0]), "v"(af[0][0]));
                    else MFMA_ACC(a4[0], f4[p & 1][k], af[0][k]);
                    if (cb < 7) f4[cb & 1][k] = W4_RD(cb < 7 ? cb : 0, k);
                    SB();
                    if (k == 0) asm volatile("v_mfma_f32_16x16x32_bf16 %0, %1, %2, 0" : "=&v"(a4[1]) : "v"(f4[p & 1][0]), "v"(af[1][0]));
                    else MFMA_ACC(a4[1], f4[p & 1][k], af[1][k]);
                    if (k == 3) lv = peek(aL2);
                    if (cb <= 3 && k == 5) SIGP(29 + cb);
                    SB();
                }
                MFMA_ACC(a4[0], wo0, af[0][7]); MFMA_ACC(a4[1], wo0, af[1][7]);
                MFMA_ACC(a4[0], wo1, af[0][8]); MFMA_ACC(a4[1], wo1, af[1][8]);
                asm volatile("s_nop 7" : "+v"(a4[0]), "+v"(a4[1]));
                u32x4 u = swap_units(pk(a4[0][0], a4[0][1]), pk(a4[0][2], a4[0][3]), pk(a4[1][0], a4[1][1]), pk(a4[1][2], a4[1][3]));
                stg128(rWS, rsoff + p * 1024, u);
                if (cb == 5) { WAITV(1); PUBLISH_(4, 8, ln < 8 ? WS_MB2 + (rt * 8 + ln) * 512 + 384 + j * 8 : 0xfffffff0u); }
                SB();
            }
        }
        PUBLISH(3, 8, ln < 8 ? WS_MB2 + (rt * 8 + ln) * 512 + 256 + j * 8 : 0xfffffff0u);
        wait_ge(L_HELP, w == 7 ? 102 : 103);
        asm volatile(".Lfk_e2:");
        if (w >= 4) finish_block(w == 7 ? 3 : w);
    }
}
}

extern "C" void kernel_launch(void* const* d_in, const int* in_sizes, int n_in, void* d_out, int out_size, void* d_ws, size_t ws_size,
                              hipStream_t stream) {
    (void)hipFuncSetAttribute((const void*)fused_kernel, hipFuncAttributeMaxDynamicSharedMemorySize, LDS_BYTES);
    fused_kernel<<<256, NWAVE * 64, LDS_BYTES, stream>>>((const u16*)d_in[0], (const u16*)d_in[1], (const u16*)d_in[2], (const u16*)d_in[3],
                                                         (const u16*)d_in[5], (const u16*)d_in[6], (const u16*)d_in[7], (const u16*)d_in[8],
                                                         (const u16*)d_in[9], (u16*)d_out, (unsigned char*)d_ws);
}
```

```cpp
#include <hip/hip_runtime.h>
#include <hip/hip_bf16.h>

namespace {
typedef unsigned short u16;
typedef unsigned u32;
typedef unsigned long long u64;
typedef short bf16x8 __attribute__((ext_vector_type(8)));
typedef short s16x4 __attribute__((ext_vector_type(4)));
typedef float f32x4 __attribute__((ext_vector_type(4)));
typedef float f32x2 __attribute__((ext_vector_type(2)));
typedef __bf16 bf16x2 __attribute__((ext_vector_type(2)));
typedef u32 u32x4 __attribute__((ext_vector_type(4)));
typedef u32 u32x2 __attribute__((ext_vector_type(2)));
#define LAS __attribute__((address_space(3)))
#define GAS __attribute__((address_space(1)))
#define DI __device__ __forceinline__
typedef __amdgpu_buffer_rsrc_t rsrc_t;

constexpr int SEQ = 2048, DM = 448, FF = 1792;

constexpr u32 E0 = 0, E1 = 28672, E2 = 57344, E3 = 86016, E4 = 114688, XX = 143360, MR = 151552, MISC = 159744;
constexpr u32 LDS_BYTES = 163840;
constexpr u32 L_SUM = MISC, L_SQ = MISC + 256, L_GATE = MISC + 512, L_SUM1 = MISC + 768, L_SQ1 = MISC + 1024, L_CS1 = MISC + 3072, GWL = MISC + 2048;
constexpr u32 WPHI = MR;
constexpr u32 XT_CB = 8192;

constexpr u32 WS_EPOCH = 0;
constexpr u32 WS_H = 65536;
constexpr u32 WS_X1 = WS_H + 32 * 57344;
constexpr u32 WS_KVP = WS_X1 + 32 * 57344;
constexpr u32 WS_KVF = WS_KVP + 16 * 32 * 8192;
constexpr u32 WS_RS = WS_KVF + 16 * 4096;
constexpr u32 WS_MB1 = WS_RS + 32 * 64 * 7168;
constexpr u32 WS_MB2 = WS_MB1 + 131072;
constexpr u32 WS_MB3 = WS_MB2 + 131072;
constexpr u32 WS_END = WS_MB3 + 131072;
constexpr u32 SPIN_CAP = 1u << 16;
constexpr int SC1 = 16;

DI f32x4 mfma(u32x4 a, u32x4 b, f32x4 c) {
    return __builtin_amdgcn_mfma_f32_16x16x32_bf16(__builtin_bit_cast(bf16x8, a), __builtin_bit_cast(bf16x8, b), c, 0, 0, 0);
}
DI u32 pk(float a, float b) { f32x2 f = {a, b}; return __builtin_bit_cast(u32, __builtin_convertvector(f, bf16x2)); }
DI u32 pk2(f32x2 f) { return __builtin_bit_cast(u32, __builtin_convertvector(f, bf16x2)); }
DI u32x4 pk8(f32x4 a, f32x4 b) { return u32x4{pk(a[0], a[1]), pk(a[2], a[3]), pk(b[0], b[1]), pk(b[2], b[3])}; }
DI float blo(u32 v) { return __builtin_bit_cast(float, v << 16); }
DI float bhi(u32 v) { return __builtin_bit_cast(float, v & 0xffff0000u); }
DI u32x4 lds128(u32 a) { return *(LAS const u32x4*)a; }
DI u32x2 lds64(u32 a) { return *(LAS const u32x2*)a; }
DI float ldsf(u32 a) { return *(LAS const float*)a; }
DI void sts128(u32 a, u32x4 v) { *(LAS u32x4*)a = v; }
DI void stsf(u32 a, float v) { *(LAS float*)a = v; }
DI u32x4 ldw2(u32 a0, u32 a1) {
    s16x4 lo = __builtin_amdgcn_ds_read_tr16_b64_v4i16((LAS s16x4*)a0);
    s16x4 hi = __builtin_amdgcn_ds_read_tr16_b64_v4i16((LAS s16x4*)a1);
    u32x2 l = __builtin_bit_cast(u32x2, lo), h = __builtin_bit_cast(u32x2, hi);
    return u32x4{l.x, l.y, h.x, h.y};
}
DI u32x4 ldw(u32 a) { return ldw2(a, a + 512); }
DI void dma16(rsrc_t rs, u32 voff, u32 soff, u32 lds) {
    asm volatile("s_mov_b32 m0, %0\n\ts_nop 0\n\tbuffer_load_dwordx4 %1, %2, %3 offen lds" ::"s"(lds), "v"(voff), "s"(rs), "s"(soff) : "memory");
}
DI void dma_pair(rsrc_t rs, u32 v0, u32 v1, u32 soff, u32 lds) {
    asm volatile("s_mov_b32 m0, %0\n\ts_nop 0\n\t"
                 "buffer_load_dwordx4 %1, %3, %4 offen lds\n\t"
                 "buffer_load_dwordx4 %2, %3, %4 offen offset:1024 lds"
                 ::"s"(lds), "v"(v0), "v"(v1), "s"(rs), "s"(soff) : "memory");
}
struct Pat { u32 v[4]; };
DI Pat mkpat(u32 a, u32 b, u32 step) { Pat p; p.v[0] = a; p.v[1] = b + (step - 1024); p.v[2] = a + 2 * (step - 1024); p.v[3] = b + 3 * (step - 1024); return p; }
DI void dma_unit(rsrc_t rs, const Pat& p, u32 soff, u32 lds) {
    asm volatile("s_mov_b32 m0, %0\n\ts_nop 0\n\t"
                 "buffer_load_dwordx4 %1, %5, %6 offen lds\n\t"
                 "buffer_load_dwordx4 %2, %5, %6 offen offset:1024 lds\n\t"
                 "buffer_load_dwordx4 %3, %5, %6 offen offset:2048 lds\n\t"
                 "buffer_load_dwordx4 %4, %5, %6 offen offset:3072 lds"
                 ::"s"(lds), "v"(p.v[0]), "v"(p.v[1]), "v"(p.v[2]), "v"(p.v[3]), "s"(rs), "s"(soff) : "memory");
}
#define WAITV(n) asm volatile("s_waitcnt vmcnt(" #n ")" ::: "memory")
#define SLEEP(n) asm volatile("s_sleep " #n ::: "memory")
#define BAR() asm volatile("s_waitcnt lgkmcnt(0)\n\ts_barrier" ::: "memory")
#define SB() __builtin_amdgcn_sched_barrier(0)
DI u32x4 ldg128(rsrc_t rs, u32 off) { return __builtin_amdgcn_raw_buffer_load_b128(rs, off, 0, SC1); }
DI u32x2 ldg64(rsrc_t rs, u32 off) { return __builtin_amdgcn_raw_buffer_load_b64(rs, off, 0, SC1); }
DI void stg128(rsrc_t rs, u32 off, u32x4 v) { __builtin_amdgcn_raw_buffer_store_b128(v, rs, off, 0, SC1); }
DI void stg64(rsrc_t rs, u32 off, u32x2 v) { __builtin_amdgcn_raw_buffer_store_b64(v, rs, off, 0, SC1); }
DI u32 san(u32 v) { return v < 0xfffffffeu ? v : 0xfffffffeu; }
DI u32 umax3(u32 a, u32 b, u32 c) { u32 m = a > b ? a : b; return m > c ? m : c; }
DI u32 fmax4(u32 m, u32x4 v) { return umax3(umax3(m, v.x, v.y), v.z, v.w); }
template <int N> DI bool has_empty(const u32x4 (&f)[N]) {
    u32 m = 0;
#pragma unroll
    for (int i = 0; i < N; ++i) m = fmax4(m, f[i]);
    return __any(m == 0xffffffffu);
}
DI f32x2 gelu2(f32x2 y, float k1, float k2, float so) {
    f32x2 t = __builtin_elementwise_fma(y * y, f32x2{k2, k2}, f32x2{k1, k1}) * y;
    f32x2 d = f32x2{__builtin_amdgcn_exp2f(t.x), __builtin_amdgcn_exp2f(t.y)} + 1.f;
    f32x2 r = {__builtin_amdgcn_rcpf(d.x), __builtin_amdgcn_rcpf(d.y)};
    return (y * so) * r;
}
constexpr float GC1 = 2.3022082f, GC2 = 0.10294324f;
DI float f_of(u32 v) { return __builtin_bit_cast(float, v); }
DI u32 u_of(float v) { return __builtin_bit_cast(u32, v); }
DI float xg_sum(float v) {
    auto r = __builtin_amdgcn_permlane32_swap(u_of(v), u_of(v), false, false);
    v = f_of(r[0]) + f_of(r[1]);
    auto s = __builtin_amdgcn_permlane16_swap(u_of(v), u_of(v), false, false);
    return f_of(s[0]) + f_of(s[1]);
}
DI float xg_max(float v) {
    auto r = __builtin_amdgcn_permlane32_swap(u_of(v), u_of(v), false, false);
    v = fmaxf(f_of(r[0]), f_of(r[1]));
    auto s = __builtin_amdgcn_permlane16_swap(u_of(v), u_of(v), false, false);
    return fmaxf(f_of(s[0]), f_of(s[1]));
}
template <int CTRL> DI float dpp(float v) {
    return __builtin_bit_cast(float, __builtin_amdgcn_update_dpp(0, __builtin_bit_cast(int, v), CTRL, 0xf, 0xf, true));
}
DI float row_sum(float v) { v += dpp<0x128>(v); v += dpp<0x124>(v); v += dpp<0x122>(v); v += dpp<0x121>(v); return v; }
DI float row_max(float v) {
    asm volatile("s_nop 1\n\tv_max_f32_dpp %0, %0, %0 row_ror:8 row_mask:0xf bank_mask:0xf\n\ts_nop 1\n\t"
                 "v_max_f32_dpp %0, %0, %0 row_ror:4 row_mask:0xf bank_mask:0xf\n\ts_nop 1\n\t"
                 "v_max_f32_dpp %0, %0, %0 row_ror:2 row_mask:0xf bank_mask:0xf\n\ts_nop 1\n\t"
                 "v_max_f32_dpp %0, %0, %0 row_ror:1 row_mask:0xf bank_mask:0xf" : "+v"(v));
    return v;
}
DI u32x4 swap_units(u32 a0, u32 a1, u32 b0, u32 b1) {
    auto r0 = __builtin_amdgcn_permlane16_swap(a0, b0, false, false);
    auto r1 = __builtin_amdgcn_permlane16_swap(a1, b1, false, false);
    return u32x4{(u32)r0[0], (u32)r1[0], (u32)r0[1], (u32)r1[1]};
}
constexpr float LOG2E = 1.4426950408889634f;
DI float diag4(f32x4 d, u32 c) { float a = (c & 1) ? d[1] : d[0], b = (c & 1) ? d[3] : d[2]; return (c & 2) ? b : a; }


#define PIN(x) asm volatile("" : "+v"(x))
constexpr int NWAVE = 12;
constexpr u32 L_PROG = MISC + 1280, L_HELP = L_PROG + 32, L_AUX = L_PROG + 36, L_LAND = L_PROG + 40, L_TAG = L_PROG + 56, L_CNT = L_PROG + 96;
DI void sigv(u32 addr, u32 val) { asm volatile("ds_write_b32 %0, %1" ::"v"(addr), "v"(val) : "memory"); }
DI void sig(u32 addr, u32 val) { asm volatile("s_mov_b64 exec, 1\n\tds_write_b32 %0, %1\n\ts_mov_b64 exec, -1" ::"v"(addr), "v"(val) : "memory"); }
DI u32 peek(u32 addr) { return *(volatile LAS u32*)addr; }
DI void wait_ge(u32 addr, u32 thr) {
    _Pragma("unroll 1") for (u32 sp = 0; sp < (SPIN_CAP << 4); ++sp) {
        u32 v;
        asm volatile("ds_read_b32 %0, %1\n\ts_waitcnt lgkmcnt(0)" : "=v"(v) : "v"(addr) : "memory");
        if (__all(v >= thr)) break;
        SLEEP(1);
    }
}

DI u32 rl(u32 v, int l) { return __builtin_amdgcn_readlane(v, l); }
DI void snap_wait(u32 aS, u32 thr, u32& sv) {
    _Pragma("unroll 1") for (u32 sp = 0; sp < (SPIN_CAP << 4); ++sp) {
        asm volatile("ds_read_b32 %0, %1\n\ts_waitcnt lgkmcnt(0)" : "=v"(sv) : "v"(aS) : "memory");
        if (__all(sv >= thr)) break;
        SLEEP(1);
    }
}
#define MFMA_ACC(acc, a, b) asm volatile("v_mfma_f32_16x16x32_bf16 %0, %1, %2, %0" : "+v"(acc) : "v"(a), "v"(b))
#define MFMA_NEW(acc, a, b) asm volatile("v_mfma_f32_16x16x32_bf16 %0, %1, %2, 0" : "=&v"(acc) : "v"(a), "v"(b))
#define MFMA_K(first, acc, a, b) do { if (first) MFMA_NEW(acc, a, b); else MFMA_ACC(acc, a, b); } while (0)
#define MFMA_DONE2(x, y) asm volatile("s_nop 7" : "+v"(x), "+v"(y))
template <int K0, bool CS = false>
DI void proj7(u32 ent, const u32 (&tr)[4], const u32x4 (&xf)[14], f32x4 (&acc)[4], u32 aS, u32& sv, u32x4 ones = u32x4{}, f32x4* cs = nullptr) {
    u32x4 f[3][4];
    u32 wa[4];
#pragma unroll
    for (int t = 0; t < 4; ++t) { wa[t] = ent + tr[t]; PIN(wa[t]); }
#pragma unroll
    for (int k = 0; k < 2; ++k)
#pragma unroll
        for (int t = 0; t < 4; ++t) f[k][t] = ldw(wa[t] + k * 4096);
    SB();
#pragma unroll
    for (int k = 0; k < 7; ++k) {
#pragma unroll
        for (int t = 0; t < 4; ++t) {
            MFMA_K(K0 + k == 0, acc[t], f[k % 3][t], xf[K0 + k]);
            if (k < 5) f[(k + 2) % 3][t] = ldw(wa[t] + (k + 2) * 4096);
            if (k == 5 && t == 0) sv = peek(aS);
            if (CS) MFMA_K(K0 + k == 0, cs[t], f[k % 3][t], ones);
            SB();
        }
    }
    asm volatile("s_nop 7" : "+v"(acc[0]), "+v"(acc[1]), "+v"(acc[2]), "+v"(acc[3]));
    if (CS) { PIN(cs[0]); PIN(cs[1]); PIN(cs[2]); PIN(cs[3]); }
}

DI void features(f32x4 a0_, f32x4 a1_, const u32x4* wpf, u32 g, u32x4& pos, u32x4& neg) {
    u32x4 qf = pk8(a0_, a1_);
    if (g == 3) { qf.z = 0; qf.w = 0; }
    f32x4 p0 = mfma(wpf[0], qf, f32x4{}) * LOG2E, p1 = mfma(wpf[1], qf, f32x4{}) * LOG2E;
    const bool v1 = g < 3;
    if (!v1) p1 = p0;
    float mx = fmaxf(fmaxf(fmaxf(p0[0], p0[1]), fmaxf(p0[2], p0[3])), fmaxf(fmaxf(p1[0], p1[1]), fmaxf(p1[2], p1[3])));
    float mn = fminf(fminf(fminf(p0[0], p0[1]), fminf(p0[2], p0[3])), fminf(fminf(p1[0], p1[1]), fminf(p1[2], p1[3])));
    mx = xg_max(mx); mn = -xg_max(-mn);
    const f32x4 d0 = p0 - mx, d1 = p1 - mx, e0 = mn - p0, e1 = mn - p1;
    f32x4 a0, a1, b0, b1;
#pragma unroll
    for (int r = 0; r < 4; ++r) {
        a0[r] = __builtin_amdgcn_exp2f(d0[r]); a1[r] = __builtin_amdgcn_exp2f(d1[r]);
        b0[r] = __builtin_amdgcn_exp2f(e0[r]); b1[r] = __builtin_amdgcn_exp2f(e1[r]);
    }
    if (!v1) { a1 = f32x4{}; b1 = f32x4{}; }
    const f32x4 sa4 = a0 + a1, sb4 = b0 + b1;
    const float ra = __builtin_amdgcn_rcpf(xg_sum((sa4[0] + sa4[1]) + (sa4[2] + sa4[3])));
    const float rb = __builtin_amdgcn_rcpf(xg_sum((sb4[0] + sb4[1]) + (sb4[2] + sb4[3])));
    pos = pk8(a0 * ra, a1 * ra);
    neg = pk8(b0 * rb, b1 * rb);
}

__global__ void __launch_bounds__(NWAVE * 64, 3)
fused_kernel(const u16* __restrict__ x, const u16* __restrict__ wqkv, const u16* __restrict__ wphi, const u16* __restrict__ wo,
             const u16* __restrict__ w1, const u16* __restrict__ w2, const u16* __restrict__ w3, const u16* __restrict__ w4,
             const u16* __restrict__ gw, u16* out, unsigned char* ws) {
    extern __shared__ __attribute__((aligned(16))) unsigned char smem[];
    const u32 tid = threadIdx.x, lane = tid & 63;
    const u32 w = __builtin_amdgcn_readfirstlane(tid >> 6);
    {
        const u32 rt0 = 8 * ((blockIdx.x & 7) >> 1) + (blockIdx.x >> 5);
        const rsrc_t rX = __builtin_amdgcn_make_buffer_rsrc((void*)x, 0, SEQ * DM * 2, 0x00020000);
        u32 l0 = lane; PIN(l0);
        const u32 xa = (rt0 * 64 + (l0 >> 3)) * 896 + (((l0 & 7) ^ (l0 >> 4)) * 16);
        const Pat pX = mkpat(xa, xa ^ 64, 8 * 896);
        dma_unit(rX, pX, (w >> 1) * 128 + (w & 1) * (32 * 896), w * 4096);
        if (w < 2) dma_unit(rX, pX, 6 * 128 + w * (32 * 896), (12 + w) * 4096);
    }
#define TOUCH(rs, off) asm volatile("s_mov_b32 m0, %0\n\ts_nop 0\n\tbuffer_load_dword %1, %2, 0 offen lds" ::"s"(MISC + 1536), "v"(off), "s"(rs) : "memory")
    if (w == 7) {
        u64 pc; int n0;
        asm volatile("s_getpc_b64 %0\n.Lfk_pc:\n\ts_mov_b32 %1, 0\n\ts_max_i32 %1, %1, .Lfk_e0-.Lfk_pc\n\ts_max_i32 %1, %1, .Lfk_e1-.Lfk_pc\n\ts_max_i32 %1, %1, .Lfk_e2-.Lfk_pc"
                     : "=s"(pc), "=s"(n0) :: "scc");
        const rsrc_t rC = __builtin_amdgcn_make_buffer_rsrc((void*)pc, 0, n0, 0x00020000);
        TOUCH(rC, lane < 12 ? ((blockIdx.x >> 3) + lane * 32) * 128 : 0x40000000u);
    }
    const u32 g = lane >> 4, c = lane & 15;
    const u32 b = blockIdx.x;
    const u32 rt = 8 * ((b & 7) >> 1) + (b >> 5), j = 4 * (b & 1) + ((b >> 3) & 3);
    const int row0 = rt * 64;
    const rsrc_t rWS = __builtin_amdgcn_make_buffer_rsrc((void*)ws, 0, WS_END, 0x00020000);
    const bool hasB = j < 7;
    const u32 psnS = hasB ? 0u : 0x40000000u;
    const u32 odd = j & 1;
#define DEF_TR() u32 lt_ = lane; PIN(lt_); const u32 q4 = (lt_ & 15) >> 2, pp = lt_ & 3; \
    const u32 trs = ((q4 >> 1) & 1) | (((lt_ >> 4) & 1) << 1), trb = (8 * (lt_ >> 4) + q4) * 128 + pp * 8
#define TRA(t) (trb + (((t) ^ trs) << 5))
    const u32x4 ONES = {0x3f803f80u, 0x3f803f80u, 0x3f803f80u, 0x3f803f80u};
    const u32x4 EMPTY = {0xffffffffu, 0xffffffffu, 0xffffffffu, 0xffffffffu};
#define LT(n) ((u32)(7 * ((n) + 1)))
    const u32 aC = L_PROG + (lane & 7) * 4;
    if (w == 10) sigv(L_PROG + (lane & 31) * 4, ((lane & 31) >= 16 && (lane & 31) < 24) ? 98 : ((lane & 31) >= 10 && (lane & 31) < 13 ? (lane & 31) + 4 : 0));
    const bool isLoader = w >= 8 && w != 10;
    if (!isLoader) { WAITV(0); BAR(); }

    auto finish_block = [&](u32 p) -> bool {
        const u32 t = p * 64 + lane;
        const u32 off = WS_RS + ((rt * 8 + j) * 8) * 7168 + t * 16;
        u32x4 q[8];
#pragma unroll
        for (int s = 0; s < 8; ++s) q[s] = ldg128(rWS, off + s * 7168);
        const u32 half = t & 1, r = (t >> 1) & 7, nn = t >> 4;
        const u32 row = 8 * j + r, kg = 2 * nn + half;
        const u32x4 x1res = ldg128(rWS, WS_X1 + rt * 57344 + (((row >> 4) * 14 + (kg >> 2)) * 64 + (kg & 3) * 16 + (row & 15)) * 16);
        const float og = 1.f - ldsf(L_GATE + row * 4);
        u32x4 o;
#pragma unroll
        for (int e = 0; e < 4; ++e) {
            f32x2 a = f32x2{blo(x1res[e]), bhi(x1res[e])} * og;
#pragma unroll
            for (int s = 0; s < 8; ++s) a += f32x2{blo(q[s][e]), bhi(q[s][e])};
            o[e] = pk2(a);
        }
        *(GAS u32x4*)((GAS unsigned char*)out + (size_t)(row0 + row) * 896 + nn * 32 + half * 16) = o;
        return true;
    };

    if (isLoader) {
        const rsrc_t rQKV = __builtin_amdgcn_make_buffer_rsrc((void*)wqkv, 0, DM * 3 * DM * 2, 0x00020000);
        const rsrc_t rWO = __builtin_amdgcn_make_buffer_rsrc((void*)wo, 0, DM * DM * 2, 0x00020000);
        const rsrc_t rW1 = __builtin_amdgcn_make_buffer_rsrc((void*)w1, 0, DM * DM * 2, 0x00020000);
        const rsrc_t rW2 = __builtin_amdgcn_make_buffer_rsrc((void*)w2, 0, DM * DM * 2, 0x00020000);
        const rsrc_t rW3 = __builtin_amdgcn_make_buffer_rsrc((void*)w3, 0, DM * FF * 2, 0x00020000);
        const rsrc_t rW4 = __builtin_amdgcn_make_buffer_rsrc((void*)w4, 0, DM * FF * 2, 0x00020000);
        const u32 l3 = w == 11 ? 2 : w - 8;
        u32 myL = L_LAND + l3 * 4, three = 3;
        PIN(myL); PIN(three);
        const u32 r8 = lane >> 3, sl = lane & 7;
        const u32 chA = sl ^ (((r8 >> 1) & 1) << 1), chB = chA ^ 4;
        const u32 jb = hasB ? j : 0;
        u32 rot = l3, pend = 0, seen = 0;
#define LANDED() asm volatile("s_mov_b64 exec, 1\n\tds_add_u32 %0, %1\n\ts_mov_b64 exec, -1" ::"v"(myL), "v"(three) : "memory")
#define POLL() do { u32 st_; asm volatile("s_getreg_b32 %0, hwreg(HW_REG_IB_STS)" : "=s"(st_)); \
            const u32 left_ = (((st_ & 15u) | ((st_ >> 18) & 48u)) + 3) >> 2; \
            _Pragma("unroll 1") while (pend > left_) { LANDED(); --pend; } } while (0)
#define ISSUED() do { ++pend; POLL(); } while (0)
#define FLUSH() do { _Pragma("unroll 1") while (pend > 0) { POLL(); SLEEP(1); } } while (0)
#define ROOM(need) do { if (__builtin_expect(!__all(seen >= (u32)(need)), 0)) { \
            _Pragma("unroll 1") for (u32 sp = 0; sp < (SPIN_CAP << 4); ++sp) { seen = peek(aC); if (__all(seen >= (u32)(need))) break; POLL(); SLEEP(1); } } seen = peek(aC); } while (0)
        u32 met = 0;
#define ROOMS(need) do { const u32 n_ = (need); if (n_ > met) { ROOM(n_ == 5 ? ((lane & 4) ? 0u : 6u) : n_); met = n_; } } while (0)
#define RUN7(k, nB, nA, rs, pat, so0, sstep, ent) do { ROOM((lane & 4) ? (nA) : (nB)); u32 u = rot; \
            _Pragma("unroll 1") for (; u < 7; u += 3) { dma_unit(rs, pat, (so0) + u * (sstep), (ent) + u * 4096); ISSUED(); } rot = u - 7; } while (0)
        const Pat pS = mkpat(r8 * 896 + chA * 16, r8 * 896 + chB * 16, 8 * 896);
        const Pat pQ = mkpat(r8 * 2688 + (chA >> 2) * 56 + (chA & 3) * 16, r8 * 2688 + (chB >> 2) * 56 + (chB & 3) * 16, 8 * 2688);
#define LD_STD(k, nB, nA, rs, half, ent) RUN7(k, nB, nA, rs, pS, (224 * (half)) * 896 + jb * 128, 32 * 896, ent)
#define LD_QKV(k, nB, nA, sec, half, ent) RUN7(k, nB, nA, rQKV, pQ, (224 * (half)) * 2688 + (sec) * 896 + 112 * j, 32 * 2688, ent)
        {
            u32 u = rot;
            dma_unit(rW1, pS, jb * 128 + u * (32 * 896), E2 + u * 4096); u += 3;
            dma_unit(rW1, pS, jb * 128 + u * (32 * 896), E2 + u * 4096); u += 3;
            WAITV(8);
            BAR();
            pend = 2;
#pragma unroll 1
            for (; u < 14; u += 3) { dma_unit(rW1, pS, jb * 128 + u * (32 * 896), E2 + u * 4096); ISSUED(); }
            rot = u - 14;
        }
        LD_QKV(3, 0, 0, 1, 0, E4);
        LD_QKV(4, 1, 1, 1, 1, E0);
        LD_QKV(5, 1, 1, 0, 0, E1);
        LD_QKV(6, 2, 2, 0, 1, E2);
        LD_STD(7, 3, 2, rW2, 0, E3);
        LD_STD(8, 0, 3, rW2, 1, E4);
        LD_QKV(9, 0, 4, 2, 0, E0);
        LD_QKV(10, 4, 0, 2, 1, E1);
        const Pat pT = mkpat(r8 * 3584 + chA * 16, r8 * 3584 + chB * 16, 8 * 3584);
        const Pat pU = mkpat((r8 + (chA >> 2) * 32) * 3584 + (chA & 3) * 16, (r8 + (chB >> 2) * 32) * 3584 + (chB & 3) * 16, 8 * 3584);
        const u32 s3f = 448 * j + odd * 64, s3h = 448 * j + (odd ? 0 : 384);
        const u32 pr = 16 * (r8 >> 2) + (r8 & 3);
        const Pat pP = mkpat(pr * 896 + chA * 16, pr * 896 + chB * 16, 4 * 896);
        const u32 s4f = (224 * j + odd * 32) * 896, s4l = (224 * j + (odd ? 0 : 192)) * 896, sWo = 56 * j * 896;
        {
            u64 tt = 0x1065a98765a987ull, tn = 0xc9555555110000ull, ts = 0x7060543201ull;
            u32 so = s3f;
#pragma unroll 1
            for (u32 ks = 0; ks < 14; ++ks) {
                ROOMS(5 + ((u32)tn & 15u));
                dma_unit(rW3, pT, so + rot * 128, ((u32)tt & 15u) * 12288 + rot * 4096); ++pend;
                if (ks % 3 == 2) POLL();
                const u32 si = (u32)ts & 15u;
                if (si) {
                    if (rot == 0) {
                        ROOMS(5 + ((0x5510000u >> (4 * (si - 1))) & 15u));
                        dma_unit(rW3, pU, s3h + (si - 1) * (64 * 3584), (u32)(0x22210e24232221ull >> (8 * (si - 1)) & 255u) * 4096); ++pend; rot = 2;
                    } else --rot;
                }
                tt >>= 4; tn >>= 4; ts >>= 4; so += 32 * 3584;
            }
        }
        {
            u64 tt = 0x1870365a2987ull, tn = 0xb99655432210ull;
            const u32 o0 = s4f + rot * (32 * 896), o2 = rot == 0 ? s4l : sWo + (rot - 1) * (28 * 896);
#pragma unroll 1
            for (u32 cb = 0; cb < 7; ++cb) {
                if (cb == 4) { tt = 0x365a29b4ull; tn = 0xedddcccbbull; }
#pragma unroll
                for (u32 t3 = 0; t3 < 3; ++t3) {
                    ROOMS(18 + ((u32)tn & 15u));
                    const u32 ld = ((u32)tt & 15u) * 12288 + rot * 4096;
                    if (t3 < 2) dma_unit(rW4, pP, o0 + t3 * (96 * 896) + cb * 128, ld);
                    else if (rot == 0) dma_unit(rW4, pP, o2 + cb * 128, ld);
                    else dma_unit(rWO, pP, o2 + cb * 128, ld);
                    ISSUED();
                    tt >>= 4; tn >>= 4;
                }
            }
        }
        FLUSH();
        wait_ge(L_HELP, 102);
        finish_block(l3);
        asm volatile(".Lfk_e0:");
    } else if (w == 10) {
        const rsrc_t rPHI = __builtin_amdgcn_make_buffer_rsrc((void*)wphi, 0, 16 * 28 * 28 * 2, 0x00020000);
        const rsrc_t rGW = __builtin_amdgcn_make_buffer_rsrc((void*)gw, 0, DM * 2, 0x00020000);
        const u32 head = 2 * j + (rt >> 4), chunk = rt & 15;
        {
            const u32 o_ = lane < 12 ? WS_MB1 + (lane >> 2) * 131072 + (rt * 8 + j) * 512 + (lane & 3) * 128 : 0x40000000u;
            asm volatile("s_mov_b32 m0, %0\n\ts_nop 0\n\tbuffer_load_dword %1, %2, 0 offen sc1 lds" ::"s"(MISC + 1536), "v"(o_), "s"(rWS) : "memory");
        }
        dma16(rGW, lane * 16, 0, GWL);
        dma_pair(rPHI, lane * 16, lane * 16, j * 3136, WPHI);
        dma_pair(rPHI, lane * 16, lane * 16, j * 3136 + 2048, WPHI + 2048);
        const u32 tag = __builtin_amdgcn_readfirstlane(__builtin_amdgcn_raw_buffer_load_b32(rWS, WS_EPOCH, 0, SC1)) + 1;
        const u32 taghi = tag * 0x9E3779B1u ^ 0x5bd1e995u;
        WAITV(0);
        sig(L_TAG, tag);
        sig(L_HELP, 1);
        const u32 me = rt * 8 + j;
        {
#define TOUCH1(off) do { const u32 o_ = (off); asm volatile("s_mov_b32 m0, %0\n\ts_nop 0\n\tbuffer_load_dword %1, %2, 0 offen sc1 lds" ::"s"(MISC + 1536), "v"(o_), "s"(rWS) : "memory"); } while (0)
            TOUCH1(lane < 56 ? WS_H + me * 7168 + lane * 128 : 0x40000000u);
            TOUCH1(lane < 56 ? WS_X1 + me * 7168 + lane * 128 : 0x40000000u);
            TOUCH1(WS_KVP + ((2 * j + (lane >> 5)) * 32 + rt) * 4096 + (lane & 31) * 128);
        }
#define PD0 28
#define PD1 8
#define POLL_ALL(off, first, gap) do { const u32 o_ = (off); SLEEP(first); _Pragma("unroll 1") for (u32 sp = 0; sp < SPIN_CAP; ++sp) { \
            u32x2 fl = ldg64(rWS, o_); if (__all(fl.x == tag && fl.y == taghi)) break; SLEEP(gap); } } while (0)
#define POLL_ALL2(off, first, gap) POLL_ALL(off, first, gap)
        {
            const int rw = (int)rt - 1 + (int)(lane >> 3);
            wait_ge(L_CNT, 4);
            POLL_ALL2(WS_MB1 + me * 512 + ((lane < 24 && (u32)rw < 32u) ? lane : 8 + (lane & 7)) * 8, PD0, PD1);
            sig(L_HELP, 100);
            wait_ge(L_CNT + 4, 4);
            POLL_ALL2(WS_MB1 + me * 512 + 256 + (lane & 7) * 8, PD0, PD1);
            sig(L_AUX, 100);
            POLL_ALL2(WS_MB3 + me * 512 + (lane & 31) * 8, 0, 12);
        }
        u32x4 pv[8];
        {
            const u32 poff = WS_KVP + (head * 32 + g) * 4096 + chunk * 256 + c * 16;
#pragma unroll
            for (int i = 0; i < 8; ++i) pv[i] = ldg128(rWS, poff + i * 16384);
            u32x4 o;
#pragma unroll
            for (int e = 0; e < 4; ++e) {
                f32x2 s = {0.f, 0.f};
#pragma unroll
                for (int i = 0; i < 8; ++i) s += f32x2{blo(pv[i][e]), bhi(pv[i][e])};
                o[e] = pk(xg_sum(s.x), xg_sum(s.y));
            }
            if (lane < 16) stg128(rWS, WS_KVF + head * 4096 + chunk * 256 + lane * 16, o);
            WAITV(0);
            if (lane < 32) stg64(rWS, WS_MB2 + (lane * 8 + j) * 512 + rt * 8, u32x2{tag, taghi});
        }
        POLL_ALL2(WS_MB2 + me * 512 + (lane & 31) * 8, PD0, 8);
#pragma unroll
        for (int i = 0; i < 8; ++i) pv[i] = ldg128(rWS, WS_KVF + 2 * j * 4096 + lane * 16 + i * 1024);
        wait_ge(aC, (lane & 4) ? 5 : 7);
#pragma unroll
        for (int i = 0; i < 8; ++i) sts128(MR + i * 1024 + lane * 16, pv[i]);
        sig(L_HELP, 101);
        wait_ge(L_CNT + 16, 8);
        POLL_ALL2(WS_MB2 + me * 512 + 384 + (lane & 7) * 8, 12, 2);
        sig(L_HELP, 102);
        wait_ge(L_CNT + 12, 8);
        POLL_ALL2(WS_MB2 + me * 512 + 256 + (lane & 7) * 8, 8, 1);
        sig(L_HELP, 103);
        asm volatile(".Lfk_e1:");
        if (b == 0 && lane == 0) __builtin_amdgcn_raw_buffer_store_b32(tag, rWS, WS_EPOCH, 0, SC1);
    } else {
        const u32 mt = w & 3, hf = w >> 2;
        const u32 myrow = 16 * mt + c;
        DEF_TR();
        const u32 myP = L_PROG + w * 4;
#define SIGP(n) sig(myP, (n))
        const u32 aS = L_PROG + (c < 13 ? c : 0) * 4;
        u32 sv = 0;
#define NEEDL(n, id) do { if (__builtin_expect(!__all(c < 10 || c > 12 || sv >= LT(n)), 0)) { snap_wait(aS, (c >= 10 && c <= 12) ? LT(n) : 0u, sv); } } while (0)
#define NEEDW(wd, n, id) do { if (__builtin_expect(rl(sv, wd) < (u32)(n), 0)) { snap_wait(aS, c == (wd) ? (u32)(n) : 0u, sv); } } while (0)
#define NEEDP(n, id) do { if (__builtin_expect(!__all(c >= 8 || sv >= (u32)(n)), 0)) { snap_wait(aS, c < 8 ? (u32)(n) : 0u, sv); } } while (0)
#define PUBLISH(word, n, off) do { WAITV(0); PUBLISH_(word, n, off); } while (0)
#define PUBLISH_(word, n, off) do { u32 o_; \
            asm volatile("s_mov_b64 exec, 1\n\tds_add_rtn_u32 %0, %1, %2\n\ts_mov_b64 exec, -1\n\ts_waitcnt lgkmcnt(0)" : "=&v"(o_) : "v"(L_CNT + (word) * 4), "v"(1u) : "memory"); \
            if (__builtin_amdgcn_readfirstlane(o_) == (n) - 1) { const u32 tg_ = peek(L_TAG); stg64(rWS, (off), u32x2{tg_, tg_ * 0x9E3779B1u ^ 0x5bd1e995u}); } } while (0)
#define NEEDT(nB, nA, id) do { const u32 t_ = c < 4 ? (u32)(nB) : (c < 8 ? (u32)(nA) : 0u); if (__builtin_expect(!__all(sv >= t_), 0)) { snap_wait(aS, t_, sv); } } while (0)
        const u32 tr[4] = {TRA(0), TRA(1), TRA(2), TRA(3)};
        u32 hoff[2], x1o[2], zoff[2];
#pragma unroll
        for (int p2 = 0; p2 < 2; ++p2) {
            const u32 nt = 2 * p2 + (g & 1);
            const u32 grp = (4 * j + nt) / 7;
            const int dlt = grp == 0 ? -1 : (grp == 1 ? 1 : (grp == 2 ? -2 : 2));
            const u32 uu = 2 * nt + (g >> 1);
            const u32 cpart = (((2 * j + (uu >> 2)) * 64 + (uu & 3) * 16) * 16) | psnS;
            const int R = row0 + myrow, Rd = R - dlt;
            hoff[p2] = (u32)Rd < (u32)SEQ ? WS_H + cpart + (Rd >> 4) * (14 * 1024) + (Rd & 15) * 16 : 0xfffffff0u;
            const u32 own = cpart + (R >> 4) * (14 * 1024) + (R & 15) * 16;
            x1o[p2] = WS_X1 + own;
            zoff[p2] = (u32)(R + dlt) < (u32)SEQ ? 0xfffffff0u : WS_H + own;
        }
        if (w == 6) {
        {
            const rsrc_t rQKV = __builtin_amdgcn_make_buffer_rsrc((void*)wqkv, 0, DM * 3 * DM * 2, 0x00020000);
            const rsrc_t rWO = __builtin_amdgcn_make_buffer_rsrc((void*)wo, 0, DM * DM * 2, 0x00020000);
            const rsrc_t rW2 = __builtin_amdgcn_make_buffer_rsrc((void*)w2, 0, DM * DM * 2, 0x00020000);
            const rsrc_t rW3 = __builtin_amdgcn_make_buffer_rsrc((void*)w3, 0, DM * FF * 2, 0x00020000);
            const rsrc_t rW4 = __builtin_amdgcn_make_buffer_rsrc((void*)w4, 0, DM * FF * 2, 0x00020000);
            TOUCH(rQKV, lane < 37 ? (b * 37 + lane) * 128 : 0x40000000u);
            TOUCH(rW2, lane < 13 ? (b * 13 + lane) * 128 : 0x40000000u);
            TOUCH(rW3, lane < 49 ? (b * 49 + lane) * 128 : 0x40000000u);
            TOUCH(rW4, lane < 49 ? (b * 49 + lane) * 128 : 0x40000000u);
            TOUCH(rWO, lane < 13 ? (b * 13 + lane) * 128 : 0x40000000u);
        }
        }

#define LOAD_XF() u32x4 xf[14]; { const u32 sx = (myrow >> 1) & 7, a0 = myrow * 128 + ((g ^ sx) * 16), a1 = a0 ^ 64; \
            _Pragma("unroll") for (int ks = 0; ks < 14; ++ks) xf[ks] = lds128((ks >> 1) * XT_CB + ((ks & 1) ? a1 : a0)); }
#define LOAD_WPF() u32x4 wpf[4]; NEEDW(8, 1, 30); _Pragma("unroll") for (int q = 0; q < 4; ++q) { \
            const u32 a = WPHI + (q >> 1) * 1568 + (4 * g + q4) * 56 + (q & 1) * 32 + pp * 8; u32x4 t = ldw2(a, a + 16 * 56); if (g == 3) { t.z = 0; t.w = 0; } wpf[q] = t; }
        constexpr u32 G_OFF[14] = {86016, 98304, 110592, 122880, 61440, 73728, 86016, 98304, 110592, 122880, 61440, 73728, 0, 12288};
        constexpr u32 S_OFF[7] = {135168, 139264, 143360, 147456, 57344, 135168, 139264};
        constexpr u32 G_LAND[14] = {87, 91, 94, 98, 102, 106, 110, 113, 117, 120, 124, 127, 130, 133};
        constexpr u32 S_LAND[7] = {88, 95, 99, 103, 107, 114, 121};
        constexpr u32 H_OFF[21] = {86016, 98304, 110592, 24576, 122880, 61440, 73728, 36864, 0, 86016, 98304, 12288, 49152, 135168, 110592, 24576, 122880, 61440, 73728, 36864, 0};
        u32x4 phq[2][2];
        if (hf == 0) {
            if (rt == 0 || rt == 31) { stg128(rWS, zoff[0], u32x4{0, 0, 0, 0}); stg128(rWS, zoff[1], u32x4{0, 0, 0, 0}); }
            LOAD_XF();
            u32x2 xres[4];
            {
                const u32 sx = (myrow >> 1) & 7;
#pragma unroll
                for (int t = 0; t < 4; ++t) xres[t] = lds64((hasB ? j : 0) * XT_CB + myrow * 128 + (((2 * t + (g >> 1)) ^ sx) * 16) + (g & 1) * 8);
            }
            SIGP(1);
            {
                u32x4 on1 = ONES; PIN(on1);
                f32x4 s0, s1;
#pragma unroll
                for (int ks = 0; ks < 14; ++ks) { MFMA_K(ks == 0, s0, on1, xf[ks]); MFMA_K(ks == 0, s1, xf[ks], xf[ks]); }
                MFMA_DONE2(s0, s1);
                const float mean = s0[0] * (1.f / DM);
                const float var = fmaxf(xg_sum(g == (c >> 2) ? diag4(s1, c) : 0.f) * (1.f / DM) - mean * mean, 0.f);
                const float rstd = __builtin_amdgcn_rsqf(var + 1e-5f);
                const float k1 = -GC1 * rstd, k2 = -GC2 * rstd * rstd * rstd;
                f32x4 a1c[4];
                NEEDL(2, 2);
                proj7<0>(E2, tr, xf, a1c, aS, sv);
                SIGP(2);
                NEEDL(3, 3);
                proj7<7>(E3, tr, xf, a1c, aS, sv);
                SIGP(3);
                NEEDT(0, 2, 4);
                u32 pkd[4][2];
#pragma unroll
                for (int t = 0; t < 4; ++t) {
                    const f32x4 cs = __builtin_bit_cast(f32x4, lds128(L_CS1 + (16 * t + 4 * g) * 4));
                    f32x2 y0 = f32x2{a1c[t][0], a1c[t][1]} - mean * f32x2{cs[0], cs[1]}, y1 = f32x2{a1c[t][2], a1c[t][3]} - mean * f32x2{cs[2], cs[3]};
                    pkd[t][0] = (pk2(gelu2(y0, k1, k2, rstd))); pkd[t][1] = (pk2(gelu2(y1, k1, k2, rstd)));
                    if (t & 1) stg128(rWS, hoff[t >> 1], swap_units(pkd[t - 1][0], pkd[t - 1][1], pkd[t][0], pkd[t][1]));
                }
            }
            {
                NEEDW(8, 1, 30);
                const int rc = (int)rt - 1 + (int)(lane >> 3);
                PUBLISH(0, 4, (lane < 24 && (u32)rc < 32u) ? WS_MB1 + (rc * 8 + (lane & 7)) * 512 + ((2 - (lane >> 3)) * 8 + j) * 8 : 0xfffffff0u);
            }
            f32x4 aq[4];
            NEEDL(6, 5);
            proj7<0>(E1, tr, xf, aq, aS, sv);
            SIGP(4);
            NEEDL(7, 6);
            proj7<7>(E2, tr, xf, aq, aS, sv);
            SIGP(5);
            u32x4 hfr[14];
            const u32 goff = (WS_H + rt * 57344 + (mt * 14 * 64 + lane) * 16) | psnS;
            NEEDW(8, 100, 7);
#pragma unroll
            for (int i = 0; i < 14; ++i) hfr[i] = ldg128(rWS, goff + i * 1024);
            f32x4 a2[4];
            NEEDL(9, 8);
            proj7<0>(E3, tr, hfr, a2, aS, sv);
            proj7<7>(E4, tr, hfr, a2, aS, sv);
            u32 pkd[4][2];
#pragma unroll
            for (int t = 0; t < 4; ++t) {
                pkd[t][0] = (pk(a2[t][0] + blo(xres[t].x), a2[t][1] + bhi(xres[t].x)));
                pkd[t][1] = (pk(a2[t][2] + blo(xres[t].y), a2[t][3] + bhi(xres[t].y)));
                if (t & 1) stg128(rWS, x1o[0] + (t >> 1) * 1024, swap_units(pkd[t - 1][0], pkd[t - 1][1], pkd[t][0], pkd[t][1]));
            }
            SIGP(6);
            PUBLISH(1, 4, lane < 8 ? WS_MB1 + (rt * 8 + lane) * 512 + 256 + j * 8 : 0xfffffff0u);
            {
                LOAD_WPF();
                SIGP(7);
                features(aq[0], aq[1], wpf, g, phq[0][0], phq[0][1]);
                features(aq[2], aq[3], wpf + 2, g, phq[1][0], phq[1][1]);
                PIN(phq[0][0]); PIN(phq[0][1]); PIN(phq[1][0]); PIN(phq[1][1]);
            }
        } else {
            LOAD_XF();
            SIGP(1);
            {
                u32 ca = E2 + TRA(mt); PIN(ca);
                f32x4 cs = {};
                NEEDL(3, 2);
#pragma unroll
                for (int k = 0; k < 14; ++k) cs = mfma(ldw(ca + k * 4096), ONES, cs);
                if (c == 0) sts128(L_CS1 + (16 * mt + 4 * g) * 4, __builtin_bit_cast(u32x4, cs));
                SIGP(2);
            }
            {
                NEEDW(8, 1, 30);
                f32x4 s0 = {};
#pragma unroll
                for (int ks = 0; ks < 14; ++ks) s0 = mfma(lds128(GWL + ks * 64 + g * 16), xf[ks], s0);
                if (g == 0) stsf(L_GATE + myrow * 4, __builtin_amdgcn_rcpf(1.f + __builtin_amdgcn_exp2f(-LOG2E * s0[0])));
            }
            const u32 wsw = ((c >> 1) & 1) | (((c >> 3) & 1) << 1);
            const u32 wrb = E2 + myrow * 128 + g * 8;
            {
                f32x4 ak[4];
                NEEDL(4, 5);
                proj7<0>(E4, tr, xf, ak, aS, sv);
                SIGP(3);
                NEEDL(5, 6);
                proj7<7>(E0, tr, xf, ak, aS, sv);
                SIGP(4);
                LOAD_WPF();
                u32x4 pos[2], neg[2];
                features(ak[0], ak[1], wpf, g, pos[0], neg[0]);
                features(ak[2], ak[3], wpf + 2, g, pos[1], neg[1]);
                NEEDT(5, 0, 32);
#pragma unroll
                for (int hd = 0; hd < 2; ++hd) {
                    *(LAS u32x2*)(wrb + hd * 8192 + ((0 ^ wsw) << 5)) = u32x2{pos[hd].x, pos[hd].y};
                    *(LAS u32x2*)(wrb + hd * 8192 + ((1 ^ wsw) << 5)) = u32x2{pos[hd].z, pos[hd].w};
                    *(LAS u32x2*)(wrb + hd * 8192 + ((2 ^ wsw) << 5)) = u32x2{neg[hd].x, neg[hd].y};
                    *(LAS u32x2*)(wrb + hd * 8192 + ((3 ^ wsw) << 5)) = u32x2{neg[hd].z, neg[hd].w};
                }
            }
            {
                f32x4 av[4];
                NEEDL(10, 10);
                proj7<0>(E0, tr, xf, av, aS, sv);
                NEEDL(11, 11);
                proj7<7>(E1, tr, xf, av, aS, sv);
#pragma unroll
                for (int t = 0; t < 4; ++t) {
                    u32x2 vv = {pk(av[t][0], av[t][1]), pk(av[t][2], av[t][3])};
                    if ((t & 1) && g == 3) vv = u32x2{0x00003f80u, 0u};
                    *(LAS u32x2*)(wrb + 16384 + ((t ^ wsw) << 5)) = vv;
                }
            }
            SIGP(5);
            NEEDT(0, 5, 13);
            {
                const u32 pa = E2 + TRA(2 * (mt >> 1)), pb = E2 + TRA(2 * (mt >> 1) + 1);
                u32x4 kp[2];
#pragma unroll
                for (int hd = 0; hd < 2; ++hd) {
                    f32x4 kv[2] = {};
                    const u32 va = E2 + 16384 + TRA(2 * hd + (mt & 1));
#pragma unroll
                    for (int ks = 0; ks < 2; ++ks) {
                        const u32x4 vf = ldw(va + ks * 4096);
                        kv[0] = mfma(ldw(pa + hd * 8192 + ks * 4096), vf, kv[0]);
                        kv[1] = mfma(ldw(pb + hd * 8192 + ks * 4096), vf, kv[1]);
                    }
                    const u32x4 t = pk8(kv[0], kv[1]);
                    kp[hd] = t;
                }
                SIGP(6);
#pragma unroll
                for (int hd = 0; hd < 2; ++hd) stg128(rWS, WS_KVP + ((2 * j + hd) * 32 + rt) * 4096 + mt * 1024 + lane * 16, kp[hd]);
                PUBLISH(2, 4, lane < 32 ? WS_MB3 + (lane * 8 + j) * 512 + rt * 8 : 0xfffffff0u);
            }
        }
        u32x4 pre[6][2];
        {
            u32x4 xv[7];
            const u32 x1off = WS_X1 + rt * 57344 + ((mt * 14 + 7 * hf) * 64 + lane) * 16;
            NEEDW(9, 100, 12);
#pragma unroll
            for (int i = 0; i < 7; ++i) xv[i] = ldg128(rWS, x1off + i * 1024);
            NEEDT(0, 5, 15);
#pragma unroll
            for (int i = 0; i < 7; ++i) sts128(((7 * hf * 4 + mt) * 64 + lane) * 16 + i * 4096, xv[i]);
        }
        {
            { u32 l_ = lane; PIN(l_); wait_ge(L_LAND + ((l_ & 3) == 3 ? 0 : (l_ & 3)) * 4, G_LAND[5]); }
            const bool w6 = w == 6;
            const u32 ub = w6 ? 0 : ((w >> 1) % 3) * 4096;
            const u32 e0 = ub + TRA(2 * (w & 1)), e1 = ub + TRA(2 * (w & 1) + 1), o0 = w6 ? TRA(2) : e0, o1 = w6 ? TRA(3) : e1;
#pragma unroll
            for (int ks = 0; ks < 6; ++ks) {
                const u32 off = w6 ? S_OFF[ks >> 1] : G_OFF[ks];
                pre[ks][0] = ldw(((ks & 1) ? o0 : e0) + off); pre[ks][1] = ldw(((ks & 1) ? o1 : e1) + off);
            }
        }
        SIGP(11);
        u32 ln = lane; PIN(ln);
        const u32 g2 = ln >> 4, c2 = ln & 15;
        const u32 trs2 = ((ln >> 3) & 1) | ((g2 & 1) << 1);
        const u32 trb2 = (8 * g2 + (c2 >> 2)) * 128 + (ln & 3) * 8;
#define TRB(t) (trb2 + (((t) ^ trs2) << 5))
        const u32 aC2 = L_PROG + (ln & 7) * 4, aL2 = L_LAND + (c2 < 3 ? c2 : 0) * 4;
        const u32 mh4 = w & 1, nt4 = w >> 1;
        u32 rsoff;
        {
            const u32 row = 16 * (2 * mh4 + (g2 & 1)) + c2;
            rsoff = WS_RS + ((rt * 8 + (row >> 3)) * 8 + j) * 7168 + (nt4 * 8 + (row & 7)) * 32 + (g2 >> 1) * 16;
        }
        constexpr u32 B_LAND[7] = {142, 151, 160, 169, 178, 187, 196};
        constexpr u32 AF_OFF[3] = {12288, 49152, 135168};

        const u32 xb = ln * 16;
        u32 wb4[3];
        wb4[0] = TRB(nt4); wb4[1] = wb4[0] + 61440; wb4[2] = wb4[0] + 122880;
        #define W4_OFF(cb, k) (H_OFF[3 * (cb) + (k) / 3] + ((k) % 3) * 4096)
#define W4_RD(cb, k) ldw(wb4[W4_OFF(cb, k) / 61440] + W4_OFF(cb, k) % 61440)
        u32x4 f4[2][7];
#define S4_PRE() do { wait_ge(aL2, B_LAND[0]); _Pragma("unroll") for (int k = 0; k < 7; ++k) f4[0][k] = W4_RD(0, k); SIGP(27); } while (0)
        if (w < 7) {
            f32x4 a3[4][2], a3s[2];
            u32x4 f[2][2], xr[2][4], on3 = ONES;
            u32 pa = ln == 0 ? myP : (ln == 1 ? myP + 64 : MISC + 1536 + ln * 4), two = ln == 1 ? 8 : 2;
            const u32 aQ = L_PROG + (c2 < 7 ? 16 + c2 : (c2 < 10 ? c2 + 3 : 10)) * 4;
            PIN(on3); PIN(pa); PIN(two);
#define S3_F(ks, t) ((ks) < 6 ? pre[(ks) < 6 ? (ks) : 0][t] : f[(ks) & 1][t])
#define S3_RX(ks, m) xr[(ks) & 1][m] = lds128(xb + ((ks) * 4 + (m)) * 1024)
#define S3_LOOP(RW, LAND) do { \
            wait_ge(aC2, 11); \
            u32 lv = 0; \
            S3_RX(0, 0); S3_RX(0, 1); S3_RX(0, 2); S3_RX(0, 3); \
            SB(); \
            _Pragma("unroll") for (int ks = 0; ks < 14; ++ks) { \
                const int kn = ks < 13 ? ks + 1 : 13; \
                if ((ks & 1) && ks < 13) { if (__builtin_expect(!__all(lv >= (u32)(98 + 4 * (ks - 1))), 0)) { wait_ge(aQ, 98 + 4 * (ks - 1)); } } \
                MFMA_K(ks == 0, a3s[0], S3_F(ks, 0), on3); \
                if (ks < 13 && kn >= 6) RW(kn, 0); \
                SB(); \
                MFMA_K(ks == 0, a3s[1], S3_F(ks, 1), on3); \
                if (ks < 13 && kn >= 6) RW(kn, 1); \
                SB(); \
                _Pragma("unroll") for (int m = 0; m < 4; ++m) { \
                    MFMA_K(ks == 0, a3[m][0], S3_F(ks, 0), xr[ks & 1][m]); \
                    if (ks < 13) S3_RX(kn, m); \
                    SB(); \
                    MFMA_K(ks == 0, a3[m][1], S3_F(ks, 1), xr[ks & 1][m]); \
                    if (m == 1 && !(ks & 1)) lv = peek(aQ); \
                    if (m == 3 && !(ks & 1)) asm volatile("ds_add_u32 %0, %1" ::"v"(pa), "v"(two) : "memory"); \
                    SB(); \
                } \
            } } while (0)
            {
                const bool w6 = w == 6;
                u32 wa[2][2];
                wa[0][0] = (w6 ? 0 : (w >> 1) * 4096) + TRB(2 * (w & 1)); wa[0][1] = (w6 ? 0 : (w >> 1) * 4096) + TRB(2 * (w & 1) + 1);
                wa[1][0] = w6 ? TRB(2) : wa[0][0]; wa[1][1] = w6 ? TRB(3) : wa[0][1];
                PIN(wa[0][0]); PIN(wa[0][1]); PIN(wa[1][0]); PIN(wa[1][1]);
#define S3_RWG(ks, t) do { u32 a_ = wa[(ks) & 1][t] + (w6 ? S_OFF[(ks) >> 1] : G_OFF[ks]); PIN(a_); f[(ks) & 1][t] = ldw(a_); } while (0)
                S3_LOOP(S3_RWG, 0);
            }
            asm volatile("s_nop 7" ::: "memory");
            SIGP(26);
            PIN(wb4[0]); PIN(wb4[1]); PIN(wb4[2]);
            S4_PRE();
            wait_ge(aC2, 26);
            float mean[4], k1[4], k2[4], so[4];
#pragma unroll
            for (int m = 0; m < 4; ++m) {
                const u32 row = 16 * m + c2;
                mean[m] = ldsf(L_SUM1 + row * 4) * (1.f / DM);
                const float var = fmaxf(ldsf(L_SQ1 + row * 4) * (1.f / DM) - mean[m] * mean[m], 0.f);
                const float rstd = __builtin_amdgcn_rsqf(var + 1e-5f);
                k1[m] = -GC1 * rstd; k2[m] = -GC2 * rstd * rstd * rstd; so[m] = rstd * (1.f - ldsf(L_GATE + row * 4));
            }
            const u32 hdst = (w < 3 ? AF_OFF[0] : (w < 6 ? AF_OFF[1] : AF_OFF[2])) + (w % 3) * 4096 + ln * 16;
#pragma unroll
            for (int m = 0; m < 4; ++m) {
                u32x4 o;
#pragma unroll
                for (int t = 0; t < 2; ++t) {
                    f32x2 y0 = f32x2{a3[m][t][0], a3[m][t][1]} - mean[m] * f32x2{a3s[t][0], a3s[t][1]};
                    f32x2 y1 = f32x2{a3[m][t][2], a3[m][t][3]} - mean[m] * f32x2{a3s[t][2], a3s[t][3]};
                    o[2 * t] = pk2(gelu2(y0, k1[m], k2[m], so[m])); o[2 * t + 1] = pk2(gelu2(y1, k1[m], k2[m], so[m]));
                }
                sts128(hdst + m * 1024, o);
            }
        } else {
            f32x4 s1[4], s2[4];
            u32x4 v[2][4], on7 = ONES;
            PIN(on7);
            wait_ge(aC2, 11);
#pragma unroll
            for (int q = 0; q < 4; ++q) v[0][q] = lds128(xb + q * 1024);
            SB();
#pragma unroll
            for (int ks = 0; ks < 14; ++ks) {
#pragma unroll
                for (int q = 0; q < 4; ++q) {
                    MFMA_K(ks == 0, s1[q], on7, v[ks & 1][q]);
                    if (ks < 13) v[(ks + 1) & 1][q] = lds128(xb + ((ks + 1) * 4 + q) * 1024);
                    SB();
                    MFMA_K(ks == 0, s2[q], v[ks & 1][q], v[ks & 1][q]);
                }
                if (!(ks & 1)) SIGP(13 + ks);
            }
            MFMA_DONE2(s1[3], s2[3]);
            if (g2 == (c2 >> 2)) {
#pragma unroll
                for (int m = 0; m < 4; ++m) { stsf(L_SUM1 + (16 * m + c2) * 4, s1[m][0]); stsf(L_SQ1 + (16 * m + c2) * 4, diag4(s2[m], c2)); }
            }
            SIGP(26);
            PIN(wb4[0]); PIN(wb4[1]); PIN(wb4[2]);
            S4_PRE();
            wait_ge(aC2, 26);
        }
        u32x4 af[2][9];
        if (hf == 0) {
            wait_ge(L_HELP, 101);
            const float gt = ldsf(L_GATE + (16 * mt + c2) * 4);
#pragma unroll
            for (int hd = 0; hd < 2; ++hd) {
                u32x4 kf[4];
#pragma unroll
                for (int i = 0; i < 4; ++i) kf[i] = lds128(MR + (hd * 4 + i) * 1024 + ln * 16);
                f32x4 o0 = mfma(kf[0], phq[hd][0], f32x4{}); o0 = mfma(kf[2], phq[hd][1], o0);
                f32x4 o1 = mfma(kf[1], phq[hd][0], f32x4{}); o1 = mfma(kf[3], phq[hd][1], o1);
                const float den = xg_sum(g2 == 3 ? o1[0] : 0.f) + 1e-6f;
                const float sc = gt * __builtin_amdgcn_rcpf(den);
                u32x4 f = pk8(o0 * sc, o1 * sc);
                if (g2 == 3) { f.z = 0; f.w = 0; }
                sts128(AF_OFF[2] + (1 + hd) * 4096 + mt * 1024 + ln * 16, f);
            }
        }
        SIGP(28);
        {
            wait_ge(aC2, 28);
            {
                u32 ab = (2 * mh4) * 1024 + ln * 16, ab2 = ab + AF_OFF[2];
                PIN(ab); PIN(ab2);
#pragma unroll
                for (int k = 0; k < 9; ++k)
#pragma unroll
                    for (int m = 0; m < 2; ++m) af[m][k] = lds128((k < 6 ? ab + AF_OFF[k / 3] : ab2) + (k % 3) * 4096 + m * 1024);
            }
            SIGP(29);
            u32 lv = peek(aL2);
#pragma unroll
            for (int cb = 1; cb < 8; ++cb) {
                const int p = cb - 1;
                if (cb < 7) { if (__builtin_expect(!__all(lv >= B_LAND[cb < 7 ? cb : 0]), 0)) { wait_ge(aL2, B_LAND[cb < 7 ? cb : 0]); } }
                const u32x4 wo0 = W4_RD(p, 7), wo1 = W4_RD(p, 8);
                f32x4 a4[2];
#pragma unroll
                for (int k = 0; k < 7; ++k) {
                    if (k == 0) asm volatile("v_mfma_f32_16x16x32_bf16 %0, %1, %2, 0" : "=&v"(a4[0]) : "v"(f4[p & 1][0]), "v"(af[0][0]));
                    else MFMA_ACC(a4[0], f4[p & 1][k], af[0][k]);
                    if (cb < 7) f4[cb & 1][k] = W4_RD(cb < 7 ? cb : 0, k);
                    SB();
                    if (k == 0) asm volatile("v_mfma_f32_16x16x32_bf16 %0, %1, %2, 0" : "=&v"(a4[1]) : "v"(f4[p & 1][0]), "v"(af[1][0]));
                    else MFMA_ACC(a4[1], f4[p & 1][k], af[1][k]);
                    if (k == 3) lv = peek(aL2);
                    if (cb <= 3 && k == 5) SIGP(29 + cb);
                    SB();
                }
                MFMA_ACC(a4[0], wo0, af[0][7]); MFMA_ACC(a4[1], wo0, af[1][7]);
                MFMA_ACC(a4[0], wo1, af[0][8]); MFMA_ACC(a4[1], wo1, af[1][8]);
                asm volatile("s_nop 7" : "+v"(a4[0]), "+v"(a4[1]));
                u32x4 u = swap_units(pk(a4[0][0], a4[0][1]), pk(a4[0][2], a4[0][3]), pk(a4[1][0], a4[1][1]), pk(a4[1][2], a4[1][3]));
                stg128(rWS, rsoff + p * 1024, u);
                if (cb == 5) { WAITV(1); PUBLISH_(4, 8, ln < 8 ? WS_MB2 + (rt * 8 + ln) * 512 + 384 + j * 8 : 0xfffffff0u); }
                SB();
            }
        }
        PUBLISH(3, 8, ln < 8 ? WS_MB2 + (rt * 8 + ln) * 512 + 256 + j * 8 : 0xfffffff0u);
        wait_ge(L_HELP, w == 7 ? 102 : 103);
        asm volatile(".Lfk_e2:");
        if (w >= 4) finish_block(w == 7 ? 3 : w);
    }
}
}

extern "C" void kernel_launch(void* const* d_in, const int* in_sizes, int n_in, void* d_out, int out_size, void* d_ws, size_t ws_size,
                              hipStream_t stream) {
    (void)hipFuncSetAttribute((const void*)fused_kernel, hipFuncAttributeMaxDynamicSharedMemorySize, LDS_BYTES);
    fused_kernel<<<256, NWAVE * 64, LDS_BYTES, stream>>>((const u16*)d_in[0], (const u16*)d_in[1], (const u16*)d_in[2], (const u16*)d_in[3],
                                                         (const u16*)d_in[5], (const u16*)d_in[6], (const u16*)d_in[7], (const u16*)d_in[8],
                                                         (const u16*)d_in[9], (u16*)d_out, (unsigned char*)d_ws);
}
```

```cpp
#include <hip/hip_runtime.h>
#include <hip/hip_bf16.h>

namespace {
typedef unsigned short u16;
typedef unsigned u32;
typedef unsigned long long u64;
typedef short bf16x8 __attribute__((ext_vector_type(8)));
typedef short s16x4 __attribute__((ext_vector_type(4)));
typedef float f32x4 __attribute__((ext_vector_type(4)));
typedef float f32x2 __attribute__((ext_vector_type(2)));
typedef __bf16 bf16x2 __attribute__((ext_vector_type(2)));
typedef u32 u32x4 __attribute__((ext_vector_type(4)));
typedef u32 u32x2 __attribute__((ext_vector_type(2)));
#define LAS __attribute__((address_space(3)))
#define GAS __attribute__((address_space(1)))
#define DI __device__ __forceinline__
typedef __amdgpu_buffer_rsrc_t rsrc_t;

constexpr int SEQ = 2048, DM = 448, FF = 1792;

constexpr u32 E0 = 0, E1 = 28672, E2 = 57344, E3 = 86016, E4 = 114688, XX = 143360, MR = 151552, MISC = 159744;
constexpr u32 LDS_BYTES = 163840;
constexpr u32 L_SUM = MISC, L_SQ = MISC + 256, L_GATE = MISC + 512, L_SUM1 = MISC + 768, L_SQ1 = MISC + 1024, L_CS1 = MISC + 3072, GWL = MISC + 2048;
constexpr u32 WPHI = MR;
constexpr u32 XT_CB = 8192;

constexpr u32 WS_EPOCH = 0;
constexpr u32 WS_H = 65536;
constexpr u32 WS_X1 = WS_H + 32 * 57344;
constexpr u32 WS_KVP = WS_X1 + 32 * 57344;
constexpr u32 WS_KVF = WS_KVP + 16 * 32 * 8192;
constexpr u32 WS_RS = WS_KVF + 16 * 4096;
constexpr u32 WS_MB1 = WS_RS + 32 * 64 * 7168;
constexpr u32 WS_MB2 = WS_MB1 + 131072;
constexpr u32 WS_MB3 = WS_MB2 + 131072;
constexpr u32 WS_END = WS_MB3 + 131072;
constexpr u32 SPIN_CAP = 1u << 16;
constexpr int SC1 = 16;

DI f32x4 mfma(u32x4 a, u32x4 b, f32x4 c) {
    return __builtin_amdgcn_mfma_f32_16x16x32_bf16(__builtin_bit_cast(bf16x8, a), __builtin_bit_cast(bf16x8, b), c, 0, 0, 0);
}
DI u32 pk(float a, float b) { f32x2 f = {a, b}; return __builtin_bit_cast(u32, __builtin_convertvector(f, bf16x2)); }
DI u32 pk2(f32x2 f) { return __builtin_bit_cast(u32, __builtin_convertvector(f, bf16x2)); }
DI u32x4 pk8(f32x4 a, f32x4 b) { return u32x4{pk(a[0], a[1]), pk(a[2], a[3]), pk(b[0], b[1]), pk(b[2], b[3])}; }
DI float blo(u32 v) { return __builtin_bit_cast(float, v << 16); }
DI float bhi(u32 v) { return __builtin_bit_cast(float, v & 0xffff0000u); }
DI u32x4 lds128(u32 a) { return *(LAS const u32x4*)a; }
DI u32x2 lds64(u32 a) { return *(LAS const u32x2*)a; }
DI float ldsf(u32 a) { return *(LAS const float*)a; }
DI void sts128(u32 a, u32x4 v) { *(LAS u32x4*)a = v; }
DI void stsf(u32 a, float v) { *(LAS float*)a = v; }
DI u32x4 ldw2(u32 a0, u32 a1) {
    s16x4 lo = __builtin_amdgcn_ds_read_tr16_b64_v4i16((LAS s16x4*)a0);
    s16x4 hi = __builtin_amdgcn_ds_read_tr16_b64_v4i16((LAS s16x4*)a1);
    u32x2 l = __builtin_bit_cast(u32x2, lo), h = __builtin_bit_cast(u32x2, hi);
    return u32x4{l.x, l.y, h.x, h.y};
}
DI u32x4 ldw(u32 a) { return ldw2(a, a + 512); }
DI void dma16(rsrc_t rs, u32 voff, u32 soff, u32 lds) {
    asm volatile("s_mov_b32 m0, %0\n\ts_nop 0\n\tbuffer_load_dwordx4 %1, %2, %3 offen lds" ::"s"(lds), "v"(voff), "s"(rs), "s"(soff) : "memory");
}
DI void dma_pair(rsrc_t rs, u32 v0, u32 v1, u32 soff, u32 lds) {
    asm volatile("s_mov_b32 m0, %0\n\ts_nop 0\n\t"
                 "buffer_load_dwordx4 %1, %3, %4 offen lds\n\t"
                 "buffer_load_dwordx4 %2, %3, %4 offen offset:1024 lds"
                 ::"s"(lds), "v"(v0), "v"(v1), "s"(rs), "s"(soff) : "memory");
}
struct Pat { u32 v[4]; };
DI Pat mkpat(u32 a, u32 b, u32 step) { Pat p; p.v[0] = a; p.v[1] = b + (step - 1024); p.v[2] = a + 2 * (step - 1024); p.v[3] = b + 3 * (step - 1024); return p; }
DI void dma_unit(rsrc_t rs, const Pat& p, u32 soff, u32 lds) {
    asm volatile("s_mov_b32 m0, %0\n\ts_nop 0\n\t"
                 "buffer_load_dwordx4 %1, %5, %6 offen lds\n\t"
                 "buffer_load_dwordx4 %2, %5, %6 offen offset:1024 lds\n\t"
                 "buffer_load_dwordx4 %3, %5, %6 offen offset:2048 lds\n\t"
                 "buffer_load_dwordx4 %4, %5, %6 offen offset:3072 lds"
                 ::"s"(lds), "v"(p.v[0]), "v"(p.v[1]), "v"(p.v[2]), "v"(p.v[3]), "s"(rs), "s"(soff) : "memory");
}
#define WAITV(n) asm volatile("s_waitcnt vmcnt(" #n ")" ::: "memory")
#define SLEEP(n) asm volatile("s_sleep " #n ::: "memory")
#define BAR() asm volatile("s_waitcnt lgkmcnt(0)\n\ts_barrier" ::: "memory")
#define SB() __builtin_amdgcn_sched_barrier(0)
DI u32x4 ldg128(rsrc_t rs, u32 off) { return __builtin_amdgcn_raw_buffer_load_b128(rs, off, 0, SC1); }
DI u32x2 ldg64(rsrc_t rs, u32 off) { return __builtin_amdgcn_raw_buffer_load_b64(rs, off, 0, SC1); }
DI void stg128(rsrc_t rs, u32 off, u32x4 v) { __builtin_amdgcn_raw_buffer_store_b128(v, rs, off, 0, SC1); }
DI void stg64(rsrc_t rs, u32 off, u32x2 v) { __builtin_amdgcn_raw_buffer_store_b64(v, rs, off, 0, SC1); }
DI u32 san(u32 v) { return v < 0xfffffffeu ? v : 0xfffffffeu; }
DI u32 umax3(u32 a, u32 b, u32 c) { u32 m = a > b ? a : b; return m > c ? m : c; }
DI u32 fmax4(u32 m, u32x4 v) { return umax3(umax3(m, v.x, v.y), v.z, v.w); }
template <int N> DI bool has_empty(const u32x4 (&f)[N]) {
    u32 m = 0;
#pragma unroll
    for (int i = 0; i < N; ++i) m = fmax4(m, f[i]);
    return __any(m == 0xffffffffu);
}
DI f32x2 gelu2(f32x2 y, float k1, float k2, float so) {
    f32x2 t = __builtin_elementwise_fma(y * y, f32x2{k2, k2}, f32x2{k1, k1}) * y;
    f32x2 d = f32x2{__builtin_amdgcn_exp2f(t.x), __builtin_amdgcn_exp2f(t.y)} + 1.f;
    f32x2 r = {__builtin_amdgcn_rcpf(d.x), __builtin_amdgcn_rcpf(d.y)};
    return (y * so) * r;
}
constexpr float GC1 = 2.3022082f, GC2 = 0.10294324f;
DI float f_of(u32 v) { return __builtin_bit_cast(float, v); }
DI u32 u_of(float v) { return __builtin_bit_cast(u32, v); }
DI float xg_sum(float v) {
    auto r = __builtin_amdgcn_permlane32_swap(u_of(v), u_of(v), false, false);
    v = f_of(r[0]) + f_of(r[1]);
    auto s = __builtin_amdgcn_permlane16_swap(u_of(v), u_of(v), false, false);
    return f_of(s[0]) + f_of(s[1]);
}
DI float xg_max(float v) {
    auto r = __builtin_amdgcn_permlane32_swap(u_of(v), u_of(v), false, false);
    v = fmaxf(f_of(r[0]), f_of(r[1]));
    auto s = __builtin_amdgcn_permlane16_swap(u_of(v), u_of(v), false, false);
    return fmaxf(f_of(s[0]), f_of(s[1]));
}
template <int CTRL> DI float dpp(float v) {
    return __builtin_bit_cast(float, __builtin_amdgcn_update_dpp(0, __builtin_bit_cast(int, v), CTRL, 0xf, 0xf, true));
}
DI float row_sum(float v) { v += dpp<0x128>(v); v += dpp<0x124>(v); v += dpp<0x122>(v); v += dpp<0x121>(v); return v; }
DI float row_max(float v) {
    asm volatile("s_nop 1\n\tv_max_f32_dpp %0, %0, %0 row_ror:8 row_mask:0xf bank_mask:0xf\n\ts_nop 1\n\t"
                 "v_max_f32_dpp %0, %0, %0 row_ror:4 row_mask:0xf bank_mask:0xf\n\ts_nop 1\n\t"
                 "v_max_f32_dpp %0, %0, %0 row_ror:2 row_mask:0xf bank_mask:0xf\n\ts_nop 1\n\t"
                 "v_max_f32_dpp %0, %0, %0 row_ror:1 row_mask:0xf bank_mask:0xf" : "+v"(v));
    return v;
}
DI u32x4 swap_units(u32 a0, u32 a1, u32 b0, u32 b1) {
    auto r0 = __builtin_amdgcn_permlane16_swap(a0, b0, false, false);
    auto r1 = __builtin_amdgcn_permlane16_swap(a1, b1, false, false);
    return u32x4{(u32)r0[0], (u32)r1[0], (u32)r0[1], (u32)r1[1]};
}
constexpr float LOG2E = 1.4426950408889634f;
DI float diag4(f32x4 d, u32 c) { float a = (c & 1) ? d[1] : d[0], b = (c & 1) ? d[3] : d[2]; return (c & 2) ? b : a; }


#define PIN(x) asm volatile("" : "+v"(x))
constexpr int NWAVE = 12;
constexpr u32 L_PROG = MISC + 1280, L_HELP = L_PROG + 32, L_AUX = L_PROG + 36, L_LAND = L_PROG + 40, L_TAG = L_PROG + 56, L_CNT = L_PROG + 96;
DI void sigv(u32 addr, u32 val) { asm volatile("ds_write_b32 %0, %1" ::"v"(addr), "v"(val) : "memory"); }
DI void sig(u32 addr, u32 val) { asm volatile("s_mov_b64 exec, 1\n\tds_write_b32 %0, %1\n\ts_mov_b64 exec, -1" ::"v"(addr), "v"(val) : "memory"); }
DI u32 peek(u32 addr) { return *(volatile LAS u32*)addr; }
DI void wait_ge(u32 addr, u32 thr) {
    _Pragma("unroll 1") for (u32 sp = 0; sp < (SPIN_CAP << 4); ++sp) {
        u32 v;
        asm volatile("ds_read_b32 %0, %1\n\ts_waitcnt lgkmcnt(0)" : "=v"(v) : "v"(addr) : "memory");
        if (__all(v >= thr)) break;
        SLEEP(1);
    }
}

DI u32 rl(u32 v, int l) { return __builtin_amdgcn_readlane(v, l); }
DI void snap_wait(u32 aS, u32 thr, u32& sv) {
    _Pragma("unroll 1") for (u32 sp = 0; sp < (SPIN_CAP << 4); ++sp) {
        asm volatile("ds_read_b32 %0, %1\n\ts_waitcnt lgkmcnt(0)" : "=v"(sv) : "v"(aS) : "memory");
        if (__all(sv >= thr)) break;
        SLEEP(1);
    }
}
#define MFMA_ACC(acc, a, b) asm volatile("v_mfma_f32_16x16x32_bf16 %0, %1, %2, %0" : "+v"(acc) : "v"(a), "v"(b))
#define MFMA_NEW(acc, a, b) asm volatile("v_mfma_f32_16x16x32_bf16 %0, %1, %2, 0" : "=&v"(acc) : "v"(a), "v"(b))
#define MFMA_K(first, acc, a, b) do { if (first) MFMA_NEW(acc, a, b); else MFMA_ACC(acc, a, b); } while (0)
#define MFMA_DONE2(x, y) asm volatile("s_nop 7" : "+v"(x), "+v"(y))
template <int K0, bool CS = false>
DI void proj7(u32 ent, const u32 (&tr)[4], const u32x4 (&xf)[14], f32x4 (&acc)[4], u32 aS, u32& sv, u32x4 ones = u32x4{}, f32x4* cs = nullptr) {
    u32x4 f[3][4];
    u32 wa[4];
#pragma unroll
    for (int t = 0; t < 4; ++t) { wa[t] = ent + tr[t]; PIN(wa[t]); }
#pragma unroll
    for (int k = 0; k < 2; ++k)
#pragma unroll
        for (int t = 0; t < 4; ++t) f[k][t] = ldw(wa[t] + k * 4096);
    SB();
#pragma unroll
    for (int k = 0; k < 7; ++k) {
#pragma unroll
        for (int t = 0; t < 4; ++t) {
            MFMA_K(K0 + k == 0, acc[t], f[k % 3][t], xf[K0 + k]);
            if (k < 5) f[(k + 2) % 3][t] = ldw(wa[t] + (k + 2) * 4096);
            if (k == 5 && t == 0) sv = peek(aS);
            if (CS) MFMA_K(K0 + k == 0, cs[t], f[k % 3][t], ones);
            SB();
        }
    }
    asm volatile("s_nop 7" : "+v"(acc[0]), "+v"(acc[1]), "+v"(acc[2]), "+v"(acc[3]));
    if (CS) { PIN(cs[0]); PIN(cs[1]); PIN(cs[2]); PIN(cs[3]); }
}

DI void features(f32x4 a0_, f32x4 a1_, const u32x4* wpf, u32 g, u32x4& pos, u32x4& neg) {
    u32x4 qf = pk8(a0_, a1_);
    if (g == 3) { qf.z = 0; qf.w = 0; }
    f32x4 p0 = mfma(wpf[0], qf, f32x4{}) * LOG2E, p1 = mfma(wpf[1], qf, f32x4{}) * LOG2E;
    const bool v1 = g < 3;
    if (!v1) p1 = p0;
    float mx = fmaxf(fmaxf(fmaxf(p0[0], p0[1]), fmaxf(p0[2], p0[3])), fmaxf(fmaxf(p1[0], p1[1]), fmaxf(p1[2], p1[3])));
    float mn = fminf(fminf(fminf(p0[0], p0[1]), fminf(p0[2], p0[3])), fminf(fminf(p1[0], p1[1]), fminf(p1[2], p1[3])));
    mx = xg_max(mx); mn = -xg_max(-mn);
    const f32x4 d0 = p0 - mx, d1 = p1 - mx, e0 = mn - p0, e1 = mn - p1;
    f32x4 a0, a1, b0, b1;
#pragma unroll
    for (int r = 0; r < 4; ++r) {
        a0[r] = __builtin_amdgcn_exp2f(d0[r]); a1[r] = __builtin_amdgcn_exp2f(d1[r]);
        b0[r] = __builtin_amdgcn_exp2f(e0[r]); b1[r] = __builtin_amdgcn_exp2f(e1[r]);
    }
    if (!v1) { a1 = f32x4{}; b1 = f32x4{}; }
    const f32x4 sa4 = a0 + a1, sb4 = b0 + b1;
    const float ra = __builtin_amdgcn_rcpf(xg_sum((sa4[0] + sa4[1]) + (sa4[2] + sa4[3])));
    const float rb = __builtin_amdgcn_rcpf(xg_sum((sb4[0] + sb4[1]) + (sb4[2] + sb4[3])));
    pos = pk8(a0 * ra, a1 * ra);
    neg = pk8(b0 * rb, b1 * rb);
}

__global__ void __launch_bounds__(NWAVE * 64, 3)
fused_kernel(const u16* __restrict__ x, const u16* __restrict__ wqkv, const u16* __restrict__ wphi, const u16* __restrict__ wo,
             const u16* __restrict__ w1, const u16* __restrict__ w2, const u16* __restrict__ w3, const u16* __restrict__ w4,
             const u16* __restrict__ gw, u16* out, unsigned char* ws) {
    extern __shared__ __attribute__((aligned(16))) unsigned char smem[];
    const u32 tid = threadIdx.x, lane = tid & 63;
    const u32 w = __builtin_amdgcn_readfirstlane(tid >> 6);
    {
        const u32 rt0 = 8 * ((blockIdx.x & 7) >> 1) + (blockIdx.x >> 5);
        const rsrc_t rX = __builtin_amdgcn_make_buffer_rsrc((void*)x, 0, SEQ * DM * 2, 0x00020000);
        u32 l0 = lane; PIN(l0);
        const u32 xa = (rt0 * 64 + (l0 >> 3)) * 896 + (((l0 & 7) ^ (l0 >> 4)) * 16);
        const Pat pX = mkpat(xa, xa ^ 64, 8 * 896);
        dma_unit(rX, pX, (w >> 1) * 128 + (w & 1) * (32 * 896), w * 4096);
        if (w < 2) dma_unit(rX, pX, 6 * 128 + w * (32 * 896), (12 + w) * 4096);
    }
#define TOUCH(rs, off) asm volatile("s_mov_b32 m0, %0\n\ts_nop 0\n\tbuffer_load_dword %1, %2, 0 offen lds" ::"s"(MISC + 1536), "v"(off), "s"(rs) : "memory")
    if (w == 7) {
        u64 pc; int n0;
        asm volatile("s_getpc_b64 %0\n.Lfk_pc:\n\ts_mov_b32 %1, 0\n\ts_max_i32 %1, %1, .Lfk_e0-.Lfk_pc\n\ts_max_i32 %1, %1, .Lfk_e1-.Lfk_pc\n\ts_max_i32 %1, %1, .Lfk_e2-.Lfk_pc"
                     : "=s"(pc), "=s"(n0) :: "scc");
        const rsrc_t rC = __builtin_amdgcn_make_buffer_rsrc((void*)pc, 0, n0, 0x00020000);
        TOUCH(rC, lane < 12 ? ((blockIdx.x >> 3) + lane * 32) * 128 : 0x40000000u);
    }
    const u32 g = lane >> 4, c = lane & 15;
    const u32 b = blockIdx.x;
    const u32 rt = 8 * ((b & 7) >> 1) + (b >> 5), j = 4 * (b & 1) + ((b >> 3) & 3);
    const int row0 = rt * 64;
    const rsrc_t rWS = __builtin_amdgcn_make_buffer_rsrc((void*)ws, 0, WS_END, 0x00020000);
    const bool hasB = j < 7;
    const u32 psnS = hasB ? 0u : 0x40000000u;
    const u32 odd = j & 1;
#define DEF_TR() u32 lt_ = lane; PIN(lt_); const u32 q4 = (lt_ & 15) >> 2, pp = lt_ & 3; \
    const u32 trs = ((q4 >> 1) & 1) | (((lt_ >> 4) & 1) << 1), trb = (8 * (lt_ >> 4) + q4) * 128 + pp * 8
#define TRA(t) (trb + (((t) ^ trs) << 5))
    const u32x4 ONES = {0x3f803f80u, 0x3f803f80u, 0x3f803f80u, 0x3f803f80u};
#define LT(n) ((u32)(7 * ((n) + 1)))
    const u32 aC = L_PROG + (lane & 7) * 4;
    if (w == 10) sigv(L_PROG + (lane & 31) * 4, ((lane & 31) >= 16 && (lane & 31) < 24) ? 98 : ((lane & 31) >= 10 && (lane & 31) < 13 ? (lane & 31) + 4 : 0));
    const bool isLoader = w >= 8 && w != 10;
    if (!isLoader) { WAITV(0); BAR(); }

    auto finish_block = [&](u32 p) -> bool {
        const u32 t = p * 64 + lane;
        const u32 off = WS_RS + ((rt * 8 + j) * 8) * 7168 + t * 16;
        u32x4 q[8];
#pragma unroll
        for (int s = 0; s < 8; ++s) q[s] = ldg128(rWS, off + s * 7168);
        const u32 half = t & 1, r = (t >> 1) & 7, nn = t >> 4;
        const u32 row = 8 * j + r, kg = 2 * nn + half;
        const u32x4 x1res = ldg128(rWS, WS_X1 + rt * 57344 + (((row >> 4) * 14 + (kg >> 2)) * 64 + (kg & 3) * 16 + (row & 15)) * 16);
        const float og = 1.f - ldsf(L_GATE + row * 4);
        u32x4 o;
#pragma unroll
        for (int e = 0; e < 4; ++e) {
            f32x2 a = f32x2{blo(x1res[e]), bhi(x1res[e])} * og;
#pragma unroll
            for (int s = 0; s < 8; ++s) a += f32x2{blo(q[s][e]), bhi(q[s][e])};
            o[e] = pk2(a);
        }
        *(GAS u32x4*)((GAS unsigned char*)out + (size_t)(row0 + row) * 896 + nn * 32 + half * 16) = o;
        return true;
    };

    if (isLoader) {
        const rsrc_t rQKV = __builtin_amdgcn_make_buffer_rsrc((void*)wqkv, 0, DM * 3 * DM * 2, 0x00020000);
        const rsrc_t rWO = __builtin_amdgcn_make_buffer_rsrc((void*)wo, 0, DM * DM * 2, 0x00020000);
        const rsrc_t rW1 = __builtin_amdgcn_make_buffer_rsrc((void*)w1, 0, DM * DM * 2, 0x00020000);
        const rsrc_t rW2 = __builtin_amdgcn_make_buffer_rsrc((void*)w2, 0, DM * DM * 2, 0x00020000);
        const rsrc_t rW3 = __builtin_amdgcn_make_buffer_rsrc((void*)w3, 0, DM * FF * 2, 0x00020000);
        const rsrc_t rW4 = __builtin_amdgcn_make_buffer_rsrc((void*)w4, 0, DM * FF * 2, 0x00020000);
        const u32 l3 = w == 11 ? 2 : w - 8;
        u32 myL = L_LAND + l3 * 4, three = 3;
        PIN(myL); PIN(three);
        const u32 r8 = lane >> 3, sl = lane & 7;
        const u32 chA = sl ^ (((r8 >> 1) & 1) << 1), chB = chA ^ 4;
        const u32 jb = hasB ? j : 0;
        u32 rot = l3, pend = 0, seen = 0;
#define LANDED() asm volatile("s_mov_b64 exec, 1\n\tds_add_u32 %0, %1\n\ts_mov_b64 exec, -1" ::"v"(myL), "v"(three) : "memory")
#define POLL() do { u32 st_; asm volatile("s_getreg_b32 %0, hwreg(HW_REG_IB_STS)" : "=s"(st_)); \
            const u32 left_ = (((st_ & 15u) | ((st_ >> 18) & 48u)) + 3) >> 2; \
            _Pragma("unroll 1") while (pend > left_) { LANDED(); --pend; } } while (0)
#define ISSUED() do { ++pend; POLL(); } while (0)
#define FLUSH() do { _Pragma("unroll 1") while (pend > 0) { POLL(); SLEEP(1); } } while (0)
#define ROOM(need) do { if (__builtin_expect(!__all(seen >= (u32)(need)), 0)) { \
            _Pragma("unroll 1") for (u32 sp = 0; sp < (SPIN_CAP << 4); ++sp) { seen = peek(aC); if (__all(seen >= (u32)(need))) break; POLL(); SLEEP(1); } } seen = peek(aC); } while (0)
        u32 met = 0;
#define ROOMS(need) do { const u32 n_ = (need); if (n_ > met) { ROOM(n_ == 5 ? ((lane & 4) ? 0u : 6u) : n_); met = n_; } } while (0)
#define RUN7(k, nB, nA, rs, pat, so0, sstep, ent) do { ROOM((lane & 4) ? (nA) : (nB)); u32 u = rot; \
            _Pragma("unroll 1") for (; u < 7; u += 3) { dma_unit(rs, pat, (so0) + u * (sstep), (ent) + u * 4096); ISSUED(); } rot = u - 7; } while (0)
        const Pat pS = mkpat(r8 * 896 + chA * 16, r8 * 896 + chB * 16, 8 * 896);
        const Pat pQ = mkpat(r8 * 2688 + (chA >> 2) * 56 + (chA & 3) * 16, r8 * 2688 + (chB >> 2) * 56 + (chB & 3) * 16, 8 * 2688);
#define LD_STD(k, nB, nA, rs, half, ent) RUN7(k, nB, nA, rs, pS, (224 * (half)) * 896 + jb * 128, 32 * 896, ent)
#define LD_QKV(k, nB, nA, sec, half, ent) RUN7(k, nB, nA, rQKV, pQ, (224 * (half)) * 2688 + (sec) * 896 + 112 * j, 32 * 2688, ent)
        {
            u32 u = rot;
            dma_unit(rW1, pS, jb * 128 + u * (32 * 896), E2 + u * 4096); u += 3;
            dma_unit(rW1, pS, jb * 128 + u * (32 * 896), E2 + u * 4096); u += 3;
            WAITV(8);
            BAR();
            pend = 2;
#pragma unroll 1
            for (; u < 14; u += 3) { dma_unit(rW1, pS, jb * 128 + u * (32 * 896), E2 + u * 4096); ISSUED(); }
            rot = u - 14;
        }
        LD_QKV(3, 0, 0, 1, 0, E4);
        LD_QKV(4, 1, 1, 1, 1, E0);
        LD_QKV(5, 1, 1, 0, 0, E1);
        LD_QKV(6, 2, 2, 0, 1, E2);
        LD_STD(7, 3, 2, rW2, 0, E3);
        LD_STD(8, 0, 3, rW2, 1, E4);
        LD_QKV(9, 0, 4, 2, 0, E0);
        LD_QKV(10, 4, 0, 2, 1, E1);
        const Pat pT = mkpat(r8 * 3584 + chA * 16, r8 * 3584 + chB * 16, 8 * 3584);
        const Pat pU = mkpat((r8 + (chA >> 2) * 32) * 3584 + (chA & 3) * 16, (r8 + (chB >> 2) * 32) * 3584 + (chB & 3) * 16, 8 * 3584);
        const u32 s3f = 448 * j + odd * 64, s3h = 448 * j + (odd ? 0 : 384);
        const u32 pr = 16 * (r8 >> 2) + (r8 & 3);
        const Pat pP = mkpat(pr * 896 + chA * 16, pr * 896 + chB * 16, 4 * 896);
        const u32 s4f = (224 * j + odd * 32) * 896, s4l = (224 * j + (odd ? 0 : 192)) * 896, sWo = 56 * j * 896;
        {
            u64 tt = 0x1065a98765a987ull, tn = 0xc9555555110000ull, ts = 0x7060543201ull;
            u32 so = s3f;
#pragma unroll 1
            for (u32 ks = 0; ks < 14; ++ks) {
                ROOMS(5 + ((u32)tn & 15u));
                dma_unit(rW3, pT, so + rot * 128, ((u32)tt & 15u) * 12288 + rot * 4096); ++pend;
                if (ks % 3 == 2) POLL();
                const u32 si = (u32)ts & 15u;
                if (si) {
                    if (rot == 0) {
                        ROOMS(5 + ((0x5510000u >> (4 * (si - 1))) & 15u));
                        dma_unit(rW3, pU, s3h + (si - 1) * (64 * 3584), (u32)(0x22210e24232221ull >> (8 * (si - 1)) & 255u) * 4096); ++pend; rot = 2;
                    } else --rot;
                }
                tt >>= 4; tn >>= 4; ts >>= 4; so += 32 * 3584;
            }
        }
        {
            u64 tt = 0x1870365a2987ull, tn = 0xb99655432210ull;
            const u32 o0 = s4f + rot * (32 * 896), o2 = rot == 0 ? s4l : sWo + (rot - 1) * (28 * 896);
#pragma unroll 1
            for (u32 cb = 0; cb < 7; ++cb) {
                if (cb == 4) { tt = 0x365a29b4ull; tn = 0xedddcccbbull; }
#pragma unroll
                for (u32 t3 = 0; t3 < 3; ++t3) {
                    ROOMS(18 + ((u32)tn & 15u));
                    const u32 ld = ((u32)tt & 15u) * 12288 + rot * 4096;
                    if (t3 < 2) dma_unit(rW4, pP, o0 + t3 * (96 * 896) + cb * 128, ld);
                    else if (rot == 0) dma_unit(rW4, pP, o2 + cb * 128, ld);
                    else dma_unit(rWO, pP, o2 + cb * 128, ld);
                    ISSUED();
                    tt >>= 4; tn >>= 4;
                }
            }
        }
        FLUSH();
        wait_ge(L_HELP, 102);
        finish_block(l3);
        asm volatile(".Lfk_e0:");
    } else if (w == 10) {
        const rsrc_t rPHI = __builtin_amdgcn_make_buffer_rsrc((void*)wphi, 0, 16 * 28 * 28 * 2, 0x00020000);
        const rsrc_t rGW = __builtin_amdgcn_make_buffer_rsrc((void*)gw, 0, DM * 2, 0x00020000);
        const u32 head = 2 * j + (rt >> 4), chunk = rt & 15;
        {
            const u32 o_ = lane < 12 ? WS_MB1 + (lane >> 2) * 131072 + (rt * 8 + j) * 512 + (lane & 3) * 128 : 0x40000000u;
            asm volatile("s_mov_b32 m0, %0\n\ts_nop 0\n\tbuffer_load_dword %1, %2, 0 offen sc1 lds" ::"s"(MISC + 1536), "v"(o_), "s"(rWS) : "memory");
        }
        dma16(rGW, lane * 16, 0, GWL);
        dma_pair(rPHI, lane * 16, lane * 16, j * 3136, WPHI);
        dma_pair(rPHI, lane * 16, lane * 16, j * 3136 + 2048, WPHI + 2048);
        const u32 tag = __builtin_amdgcn_readfirstlane(__builtin_amdgcn_raw_buffer_load_b32(rWS, WS_EPOCH, 0, SC1)) + 1;
        const u32 taghi = tag * 0x9E3779B1u ^ 0x5bd1e995u;
        WAITV(0);
        sig(L_TAG, tag);
        sig(L_HELP, 1);
        const u32 me = rt * 8 + j;
#define PD0 28
#define PD1 8
#define POLL_ALL(off, first, gap) do { const u32 o_ = (off); SLEEP(first); _Pragma("unroll 1") for (u32 sp = 0; sp < SPIN_CAP; ++sp) { \
            u32x2 fl = ldg64(rWS, o_); if (__all(fl.x == tag && fl.y == taghi)) break; SLEEP(gap); } \
            __builtin_amdgcn_fence(__ATOMIC_ACQUIRE, "wavefront");   } while (0)
#define POLL_ALL2(off, first, gap) POLL_ALL(off, first, gap)
        {
            const int rw = (int)rt - 1 + (int)(lane >> 3);
            wait_ge(L_CNT, 4);
            POLL_ALL2(WS_MB1 + me * 512 + ((lane < 24 && (u32)rw < 32u) ? lane : 8 + (lane & 7)) * 8, PD0, PD1);
            sig(L_HELP, 100);
            wait_ge(L_CNT + 4, 4);
            POLL_ALL2(WS_MB1 + me * 512 + 256 + (lane & 7) * 8, PD0, PD1);
            sig(L_AUX, 100);
            POLL_ALL2(WS_MB3 + me * 512 + (lane & 31) * 8, 0, 12);
        }
        u32x4 pv[8];
        {
            const u32 poff = WS_KVP + (head * 32 + g) * 4096 + chunk * 256 + c * 16;
#pragma unroll
            for (int i = 0; i < 8; ++i) pv[i] = ldg128(rWS, poff + i * 16384);
            u32x4 o;
#pragma unroll
            for (int e = 0; e < 4; ++e) {
                f32x2 s = {0.f, 0.f};
#pragma unroll
                for (int i = 0; i < 8; ++i) s += f32x2{blo(pv[i][e]), bhi(pv[i][e])};
                o[e] = pk(xg_sum(s.x), xg_sum(s.y));
            }
            if (lane < 16) stg128(rWS, WS_KVF + head * 4096 + chunk * 256 + lane * 16, o);
            WAITV(0);
            if (lane < 32) stg64(rWS, WS_MB2 + (lane * 8 + j) * 512 + rt * 8, u32x2{tag, taghi});
        }
        POLL_ALL2(WS_MB2 + me * 512 + (lane & 31) * 8, PD0, 8);
#pragma unroll
        for (int i = 0; i < 8; ++i) pv[i] = ldg128(rWS, WS_KVF + 2 * j * 4096 + lane * 16 + i * 1024);
        wait_ge(aC, (lane & 4) ? 5 : 7);
#pragma unroll
        for (int i = 0; i < 8; ++i) sts128(MR + i * 1024 + lane * 16, pv[i]);
        sig(L_HELP, 101);
        wait_ge(L_CNT + 16, 8);
        POLL_ALL2(WS_MB2 + me * 512 + 384 + (lane & 7) * 8, 12, 2);
        sig(L_HELP, 102);
        wait_ge(L_CNT + 12, 8);
        POLL_ALL2(WS_MB2 + me * 512 + 256 + (lane & 7) * 8, 8, 1);
        sig(L_HELP, 103);
        asm volatile(".Lfk_e1:");
        if (b == 0 && lane == 0) __builtin_amdgcn_raw_buffer_store_b32(tag, rWS, WS_EPOCH, 0, SC1);
    } else {
        const u32 mt = w & 3, hf = w >> 2;
        const u32 myrow = 16 * mt + c;
        DEF_TR();
        const u32 myP = L_PROG + w * 4;
#define SIGP(n) sig(myP, (n))
        const u32 aS = L_PROG + (c < 13 ? c : 0) * 4;
        u32 sv = 0;
#define NEEDL(n, id) do { if (__builtin_expect(!__all(c < 10 || c > 12 || sv >= LT(n)), 0)) { snap_wait(aS, (c >= 10 && c <= 12) ? LT(n) : 0u, sv); } } while (0)
#define NEEDW(wd, n, id) do { if (__builtin_expect(rl(sv, wd) < (u32)(n), 0)) { snap_wait(aS, c == (wd) ? (u32)(n) : 0u, sv); } } while (0)
#define NEEDP(n, id) do { if (__builtin_expect(!__all(c >= 8 || sv >= (u32)(n)), 0)) { snap_wait(aS, c < 8 ? (u32)(n) : 0u, sv); } } while (0)
#define PUBLISH(word, n, off) do { WAITV(0); PUBLISH_(word, n, off); } while (0)
#define PUBLISH_(word, n, off) do { u32 o_; \
            asm volatile("s_mov_b64 exec, 1\n\tds_add_rtn_u32 %0, %1, %2\n\ts_mov_b64 exec, -1\n\ts_waitcnt lgkmcnt(0)" : "=&v"(o_) : "v"(L_CNT + (word) * 4), "v"(1u) : "memory"); \
            if (__builtin_amdgcn_readfirstlane(o_) == (n) - 1) { const u32 tg_ = peek(L_TAG); stg64(rWS, (off), u32x2{tg_, tg_ * 0x9E3779B1u ^ 0x5bd1e995u}); } } while (0)
#define NEEDT(nB, nA, id) do { const u32 t_ = c < 4 ? (u32)(nB) : (c < 8 ? (u32)(nA) : 0u); if (__builtin_expect(!__all(sv >= t_), 0)) { snap_wait(aS, t_, sv); } } while (0)
        const u32 tr[4] = {TRA(0), TRA(1), TRA(2), TRA(3)};
        u32 hoff[2], x1o[2], zoff[2];
#pragma unroll
        for (int p2 = 0; p2 < 2; ++p2) {
            const u32 nt = 2 * p2 + (g & 1);
            const u32 grp = (4 * j + nt) / 7;
            const int dlt = grp == 0 ? -1 : (grp == 1 ? 1 : (grp == 2 ? -2 : 2));
            const u32 uu = 2 * nt + (g >> 1);
            const u32 cpart = (((2 * j + (uu >> 2)) * 64 + (uu & 3) * 16) * 16) | psnS;
            const int R = row0 + myrow, Rd = R - dlt;
            hoff[p2] = (u32)Rd < (u32)SEQ ? WS_H + cpart + (Rd >> 4) * (14 * 1024) + (Rd & 15) * 16 : 0xfffffff0u;
            const u32 own = cpart + (R >> 4) * (14 * 1024) + (R & 15) * 16;
            x1o[p2] = WS_X1 + own;
            zoff[p2] = (u32)(R + dlt) < (u32)SEQ ? 0xfffffff0u : WS_H + own;
        }
        if (w == 6) {
        {
            const rsrc_t rQKV = __builtin_amdgcn_make_buffer_rsrc((void*)wqkv, 0, DM * 3 * DM * 2, 0x00020000);
            const rsrc_t rWO = __builtin_amdgcn_make_buffer_rsrc((void*)wo, 0, DM * DM * 2, 0x00020000);
            const rsrc_t rW2 = __builtin_amdgcn_make_buffer_rsrc((void*)w2, 0, DM * DM * 2, 0x00020000);
            const rsrc_t rW3 = __builtin_amdgcn_make_buffer_rsrc((void*)w3, 0, DM * FF * 2, 0x00020000);
            const rsrc_t rW4 = __builtin_amdgcn_make_buffer_rsrc((void*)w4, 0, DM * FF * 2, 0x00020000);
            TOUCH(rQKV, lane < 37 ? (b * 37 + lane) * 128 : 0x40000000u);
            TOUCH(rW2, lane < 13 ? (b * 13 + lane) * 128 : 0x40000000u);
            TOUCH(rW3, lane < 49 ? (b * 49 + lane) * 128 : 0x40000000u);
            TOUCH(rW4, lane < 49 ? (b * 49 + lane) * 128 : 0x40000000u);
            TOUCH(rWO, lane < 13 ? (b * 13 + lane) * 128 : 0x40000000u);
        }
        }

#define LOAD_XF() u32x4 xf[14]; { const u32 sx = (myrow >> 1) & 7, a0 = myrow * 128 + ((g ^ sx) * 16), a1 = a0 ^ 64; \
            _Pragma("unroll") for (int ks = 0; ks < 14; ++ks) xf[ks] = lds128((ks >> 1) * XT_CB + ((ks & 1) ? a1 : a0)); }
#define LOAD_WPF() u32x4 wpf[4]; NEEDW(8, 1, 30); _Pragma("unroll") for (int q = 0; q < 4; ++q) { \
            const u32 a = WPHI + (q >> 1) * 1568 + (4 * g + q4) * 56 + (q & 1) * 32 + pp * 8; u32x4 t = ldw2(a, a + 16 * 56); if (g == 3) { t.z = 0; t.w = 0; } wpf[q] = t; }
        constexpr u32 G_OFF[14] = {86016, 98304, 110592, 122880, 61440, 73728, 86016, 98304, 110592, 122880, 61440, 73728, 0, 12288};
        constexpr u32 S_OFF[7] = {135168, 139264, 143360, 147456, 57344, 135168, 139264};
        constexpr u32 G_LAND[14] = {87, 91, 94, 98, 102, 106, 110, 113, 117, 120, 124, 127, 130, 133};
        constexpr u32 S_LAND[7] = {88, 95, 99, 103, 107, 114, 121};
        constexpr u32 H_OFF[21] = {86016, 98304, 110592, 24576, 122880, 61440, 73728, 36864, 0, 86016, 98304, 12288, 49152, 135168, 110592, 24576, 122880, 61440, 73728, 36864, 0};
        u32x4 phq[2][2];
        if (hf == 0) {
            if (rt == 0 || rt == 31) { stg128(rWS, zoff[0], u32x4{0, 0, 0, 0}); stg128(rWS, zoff[1], u32x4{0, 0, 0, 0}); }
            LOAD_XF();
            u32x2 xres[4];
            {
                const u32 sx = (myrow >> 1) & 7;
#pragma unroll
                for (int t = 0; t < 4; ++t) xres[t] = lds64((hasB ? j : 0) * XT_CB + myrow * 128 + (((2 * t + (g >> 1)) ^ sx) * 16) + (g & 1) * 8);
            }
            SIGP(1);
            {
                u32x4 on1 = ONES; PIN(on1);
                f32x4 s0, s1;
#pragma unroll
                for (int ks = 0; ks < 14; ++ks) { MFMA_K(ks == 0, s0, on1, xf[ks]); MFMA_K(ks == 0, s1, xf[ks], xf[ks]); }
                MFMA_DONE2(s0, s1);
                const float mean = s0[0] * (1.f / DM);
                const float var = fmaxf(xg_sum(g == (c >> 2) ? diag4(s1, c) : 0.f) * (1.f / DM) - mean * mean, 0.f);
                const float rstd = __builtin_amdgcn_rsqf(var + 1e-5f);
                const float k1 = -GC1 * rstd, k2 = -GC2 * rstd * rstd * rstd;
                f32x4 a1c[4];
                NEEDL(2, 2);
                proj7<0>(E2, tr, xf, a1c, aS, sv);
                SIGP(2);
                NEEDL(3, 3);
                proj7<7>(E3, tr, xf, a1c, aS, sv);
                SIGP(3);
                NEEDT(0, 2, 4);
                u32 pkd[4][2];
#pragma unroll
                for (int t = 0; t < 4; ++t) {
                    const f32x4 cs = __builtin_bit_cast(f32x4, lds128(L_CS1 + (16 * t + 4 * g) * 4));
                    f32x2 y0 = f32x2{a1c[t][0], a1c[t][1]} - mean * f32x2{cs[0], cs[1]}, y1 = f32x2{a1c[t][2], a1c[t][3]} - mean * f32x2{cs[2], cs[3]};
                    pkd[t][0] = (pk2(gelu2(y0, k1, k2, rstd))); pkd[t][1] = (pk2(gelu2(y1, k1, k2, rstd)));
                    if (t & 1) stg128(rWS, hoff[t >> 1], swap_units(pkd[t - 1][0], pkd[t - 1][1], pkd[t][0], pkd[t][1]));
                }
            }
            {
                NEEDW(8, 1, 30);
                const int rc = (int)rt - 1 + (int)(lane >> 3);
                PUBLISH(0, 4, (lane < 24 && (u32)rc < 32u) ? WS_MB1 + (rc * 8 + (lane & 7)) * 512 + ((2 - (lane >> 3)) * 8 + j) * 8 : 0xfffffff0u);
            }
            f32x4 aq[4];
            NEEDL(6, 5);
            proj7<0>(E1, tr, xf, aq, aS, sv);
            SIGP(4);
            NEEDL(7, 6);
            proj7<7>(E2, tr, xf, aq, aS, sv);
            SIGP(5);
            u32x4 hfr[14];
            const u32 goff = (WS_H + rt * 57344 + (mt * 14 * 64 + lane) * 16) | psnS;
            NEEDW(8, 100, 7);
#pragma unroll
            for (int i = 0; i < 14; ++i) hfr[i] = ldg128(rWS, goff + i * 1024);
            f32x4 a2[4];
            NEEDL(9, 8);
            proj7<0>(E3, tr, hfr, a2, aS, sv);
            proj7<7>(E4, tr, hfr, a2, aS, sv);
            u32 pkd[4][2];
#pragma unroll
            for (int t = 0; t < 4; ++t) {
                pkd[t][0] = (pk(a2[t][0] + blo(xres[t].x), a2[t][1] + bhi(xres[t].x)));
                pkd[t][1] = (pk(a2[t][2] + blo(xres[t].y), a2[t][3] + bhi(xres[t].y)));
                if (t & 1) stg128(rWS, x1o[0] + (t >> 1) * 1024, swap_units(pkd[t - 1][0], pkd[t - 1][1], pkd[t][0], pkd[t][1]));
            }
            SIGP(6);
            PUBLISH(1, 4, lane < 8 ? WS_MB1 + (rt * 8 + lane) * 512 + 256 + j * 8 : 0xfffffff0u);
            {
                LOAD_WPF();
                SIGP(7);
                features(aq[0], aq[1], wpf, g, phq[0][0], phq[0][1]);
                features(aq[2], aq[3], wpf + 2, g, phq[1][0], phq[1][1]);
                PIN(phq[0][0]); PIN(phq[0][1]); PIN(phq[1][0]); PIN(phq[1][1]);
            }
        } else {
            LOAD_XF();
            SIGP(1);
            {
                u32 ca = E2 + TRA(mt); PIN(ca);
                f32x4 cs = {};
                NEEDL(3, 2);
#pragma unroll
                for (int k = 0; k < 14; ++k) cs = mfma(ldw(ca + k * 4096), ONES, cs);
                if (c == 0) sts128(L_CS1 + (16 * mt + 4 * g) * 4, __builtin_bit_cast(u32x4, cs));
                SIGP(2);
            }
            {
                NEEDW(8, 1, 30);
                f32x4 s0 = {};
#pragma unroll
                for (int ks = 0; ks < 14; ++ks) s0 = mfma(lds128(GWL + ks * 64 + g * 16), xf[ks], s0);
                if (g == 0) stsf(L_GATE + myrow * 4, __builtin_amdgcn_rcpf(1.f + __builtin_amdgcn_exp2f(-LOG2E * s0[0])));
            }
            const u32 wsw = ((c >> 1) & 1) | (((c >> 3) & 1) << 1);
            const u32 wrb = E2 + myrow * 128 + g * 8;
            {
                f32x4 ak[4];
                NEEDL(4, 5);
                proj7<0>(E4, tr, xf, ak, aS, sv);
                SIGP(3);
                NEEDL(5, 6);
                proj7<7>(E0, tr, xf, ak, aS, sv);
                SIGP(4);
                LOAD_WPF();
                u32x4 pos[2], neg[2];
                features(ak[0], ak[1], wpf, g, pos[0], neg[0]);
                features(ak[2], ak[3], wpf + 2, g, pos[1], neg[1]);
                NEEDT(5, 0, 32);
#pragma unroll
                for (int hd = 0; hd < 2; ++hd) {
                    *(LAS u32x2*)(wrb + hd * 8192 + ((0 ^ wsw) << 5)) = u32x2{pos[hd].x, pos[hd].y};
                    *(LAS u32x2*)(wrb + hd * 8192 + ((1 ^ wsw) << 5)) = u32x2{pos[hd].z, pos[hd].w};
                    *(LAS u32x2*)(wrb + hd * 8192 + ((2 ^ wsw) << 5)) = u32x2{neg[hd].x, neg[hd].y};
                    *(LAS u32x2*)(wrb + hd * 8192 + ((3 ^ wsw) << 5)) = u32x2{neg[hd].z, neg[hd].w};
                }
            }
            {
                f32x4 av[4];
                NEEDL(10, 10);
                proj7<0>(E0, tr, xf, av, aS, sv);
                NEEDL(11, 11);
                proj7<7>(E1, tr, xf, av, aS, sv);
#pragma unroll
                for (int t = 0; t < 4; ++t) {
                    u32x2 vv = {pk(av[t][0], av[t][1]), pk(av[t][2], av[t][3])};
                    if ((t & 1) && g == 3) vv = u32x2{0x00003f80u, 0u};
                    *(LAS u32x2*)(wrb + 16384 + ((t ^ wsw) << 5)) = vv;
                }
            }
            SIGP(5);
            NEEDT(0, 5, 13);
            {
                const u32 pa = E2 + TRA(2 * (mt >> 1)), pb = E2 + TRA(2 * (mt >> 1) + 1);
                u32x4 kp[2];
#pragma unroll
                for (int hd = 0; hd < 2; ++hd) {
                    f32x4 kv[2] = {};
                    const u32 va = E2 + 16384 + TRA(2 * hd + (mt & 1));
#pragma unroll
                    for (int ks = 0; ks < 2; ++ks) {
                        const u32x4 vf = ldw(va + ks * 4096);
                        kv[0] = mfma(ldw(pa + hd * 8192 + ks * 4096), vf, kv[0]);
                        kv[1] = mfma(ldw(pb + hd * 8192 + ks * 4096), vf, kv[1]);
                    }
                    const u32x4 t = pk8(kv[0], kv[1]);
                    kp[hd] = t;
                }
                SIGP(6);
#pragma unroll
                for (int hd = 0; hd < 2; ++hd) stg128(rWS, WS_KVP + ((2 * j + hd) * 32 + rt) * 4096 + mt * 1024 + lane * 16, kp[hd]);
                PUBLISH(2, 4, lane < 32 ? WS_MB3 + (lane * 8 + j) * 512 + rt * 8 : 0xfffffff0u);
            }
        }
        u32x4 pre[6][2];
        {
            u32x4 xv[7];
            const u32 x1off = WS_X1 + rt * 57344 + ((mt * 14 + 7 * hf) * 64 + lane) * 16;
            NEEDW(9, 100, 12);
#pragma unroll
            for (int i = 0; i < 7; ++i) xv[i] = ldg128(rWS, x1off + i * 1024);
            NEEDT(0, 5, 15);
#pragma unroll
            for (int i = 0; i < 7; ++i) sts128(((7 * hf * 4 + mt) * 64 + lane) * 16 + i * 4096, xv[i]);
        }
        {
            { u32 l_ = lane; PIN(l_); wait_ge(L_LAND + ((l_ & 3) == 3 ? 0 : (l_ & 3)) * 4, G_LAND[5]); }
            const bool w6 = w == 6;
            const u32 ub = w6 ? 0 : ((w >> 1) % 3) * 4096;
            const u32 e0 = ub + TRA(2 * (w & 1)), e1 = ub + TRA(2 * (w & 1) + 1), o0 = w6 ? TRA(2) : e0, o1 = w6 ? TRA(3) : e1;
#pragma unroll
            for (int ks = 0; ks < 6; ++ks) {
                const u32 off = w6 ? S_OFF[ks >> 1] : G_OFF[ks];
                pre[ks][0] = ldw(((ks & 1) ? o0 : e0) + off); pre[ks][1] = ldw(((ks & 1) ? o1 : e1) + off);
            }
        }
        SIGP(11);
        u32 ln = lane; PIN(ln);
        const u32 g2 = ln >> 4, c2 = ln & 15;
        const u32 trs2 = ((ln >> 3) & 1) | ((g2 & 1) << 1);
        const u32 trb2 = (8 * g2 + (c2 >> 2)) * 128 + (ln & 3) * 8;
#define TRB(t) (trb2 + (((t) ^ trs2) << 5))
        const u32 aC2 = L_PROG + (ln & 7) * 4, aL2 = L_LAND + (c2 < 3 ? c2 : 0) * 4;
        const u32 mh4 = w & 1, nt4 = w >> 1;
        u32 rsoff;
        {
            const u32 row = 16 * (2 * mh4 + (g2 & 1)) + c2;
            rsoff = WS_RS + ((rt * 8 + (row >> 3)) * 8 + j) * 7168 + (nt4 * 8 + (row & 7)) * 32 + (g2 >> 1) * 16;
        }
        constexpr u32 B_LAND[7] = {142, 151, 160, 169, 178, 187, 196};
        constexpr u32 AF_OFF[3] = {12288, 49152, 135168};

        const u32 xb = ln * 16;
        u32 wb4[3];
        wb4[0] = TRB(nt4); wb4[1] = wb4[0] + 61440; wb4[2] = wb4[0] + 122880;
        #define W4_OFF(cb, k) (H_OFF[3 * (cb) + (k) / 3] + ((k) % 3) * 4096)
#define W4_RD(cb, k) ldw(wb4[W4_OFF(cb, k) / 61440] + W4_OFF(cb, k) % 61440)
        u32x4 f4[2][7];
#define S4_PRE() do { wait_ge(aL2, B_LAND[0]); _Pragma("unroll") for (int k = 0; k < 7; ++k) f4[0][k] = W4_RD(0, k); SIGP(27); } while (0)
        if (w < 7) {
            f32x4 a3[4][2], a3s[2];
            u32x4 f[2][2], xr[2][4], on3 = ONES;
            u32 pa = ln == 0 ? myP : (ln == 1 ? myP + 64 : MISC + 1536 + ln * 4), two = ln == 1 ? 8 : 2;
            const u32 aQ = L_PROG + (c2 < 7 ? 16 + c2 : (c2 < 10 ? c2 + 3 : 10)) * 4;
            PIN(on3); PIN(pa); PIN(two);
#define S3_F(ks, t) ((ks) < 6 ? pre[(ks) < 6 ? (ks) : 0][t] : f[(ks) & 1][t])
#define S3_RX(ks, m) xr[(ks) & 1][m] = lds128(xb + ((ks) * 4 + (m)) * 1024)
#define S3_LOOP(RW, LAND) do { \
            wait_ge(aC2, 11); \
            u32 lv = 0; \
            S3_RX(0, 0); S3_RX(0, 1); S3_RX(0, 2); S3_RX(0, 3); \
            SB(); \
            _Pragma("unroll") for (int ks = 0; ks < 14; ++ks) { \
                const int kn = ks < 13 ? ks + 1 : 13; \
                if ((ks & 1) && ks < 13) { if (__builtin_expect(!__all(lv >= (u32)(98 + 4 * (ks - 1))), 0)) { wait_ge(aQ, 98 + 4 * (ks - 1)); } } \
                MFMA_K(ks == 0, a3s[0], S3_F(ks, 0), on3); \
                if (ks < 13 && kn >= 6) RW(kn, 0); \
                SB(); \
                MFMA_K(ks == 0, a3s[1], S3_F(ks, 1), on3); \
                if (ks < 13 && kn >= 6) RW(kn, 1); \
                SB(); \
                _Pragma("unroll") for (int m = 0; m < 4; ++m) { \
                    MFMA_K(ks == 0, a3[m][0], S3_F(ks, 0), xr[ks & 1][m]); \
                    if (ks < 13) S3_RX(kn, m); \
                    SB(); \
                    MFMA_K(ks == 0, a3[m][1], S3_F(ks, 1), xr[ks & 1][m]); \
                    if (m == 1 && !(ks & 1)) lv = peek(aQ); \
                    if (m == 3 && !(ks & 1)) asm volatile("ds_add_u32 %0, %1" ::"v"(pa), "v"(two) : "memory"); \
                    SB(); \
                } \
            } } while (0)
            {
                const bool w6 = w == 6;
                u32 wa[2][2];
                wa[0][0] = (w6 ? 0 : (w >> 1) * 4096) + TRB(2 * (w & 1)); wa[0][1] = (w6 ? 0 : (w >> 1) * 4096) + TRB(2 * (w & 1) + 1);
                wa[1][0] = w6 ? TRB(2) : wa[0][0]; wa[1][1] = w6 ? TRB(3) : wa[0][1];
                PIN(wa[0][0]); PIN(wa[0][1]); PIN(wa[1][0]); PIN(wa[1][1]);
#define S3_RWG(ks, t) do { u32 a_ = wa[(ks) & 1][t] + (w6 ? S_OFF[(ks) >> 1] : G_OFF[ks]); PIN(a_); f[(ks) & 1][t] = ldw(a_); } while (0)
                S3_LOOP(S3_RWG, 0);
            }
            asm volatile("s_nop 7" ::: "memory");
            SIGP(26);
            PIN(wb4[0]); PIN(wb4[1]); PIN(wb4[2]);
            S4_PRE();
            wait_ge(aC2, 26);
            float mean[4], k1[4], k2[4], so[4];
#pragma unroll
            for (int m = 0; m < 4; ++m) {
                const u32 row = 16 * m + c2;
                mean[m] = ldsf(L_SUM1 + row * 4) * (1.f / DM);
                const float var = fmaxf(ldsf(L_SQ1 + row * 4) * (1.f / DM) - mean[m] * mean[m], 0.f);
                const float rstd = __builtin_amdgcn_rsqf(var + 1e-5f);
                k1[m] = -GC1 * rstd; k2[m] = -GC2 * rstd * rstd * rstd; so[m] = rstd * (1.f - ldsf(L_GATE + row * 4));
            }
            const u32 hdst = (w < 3 ? AF_OFF[0] : (w < 6 ? AF_OFF[1] : AF_OFF[2])) + (w % 3) * 4096 + ln * 16;
#pragma unroll
            for (int m = 0; m < 4; ++m) {
                u32x4 o;
#pragma unroll
                for (int t = 0; t < 2; ++t) {
                    f32x2 y0 = f32x2{a3[m][t][0], a3[m][t][1]} - mean[m] * f32x2{a3s[t][0], a3s[t][1]};
                    f32x2 y1 = f32x2{a3[m][t][2], a3[m][t][3]} - mean[m] * f32x2{a3s[t][2], a3s[t][3]};
                    o[2 * t] = pk2(gelu2(y0, k1[m], k2[m], so[m])); o[2 * t + 1] = pk2(gelu2(y1, k1[m], k2[m], so[m]));
                }
                sts128(hdst + m * 1024, o);
            }
        } else {
            f32x4 s1[4], s2[4];
            u32x4 v[2][4], on7 = ONES;
            PIN(on7);
            wait_ge(aC2, 11);
#pragma unroll
            for (int q = 0; q < 4; ++q) v[0][q] = lds128(xb + q * 1024);
            SB();
#pragma unroll
            for (int ks = 0; ks < 14; ++ks) {
#pragma unroll
                for (int q = 0; q < 4; ++q) {
                    MFMA_K(ks == 0, s1[q], on7, v[ks & 1][q]);
                    if (ks < 13) v[(ks + 1) & 1][q] = lds128(xb + ((ks + 1) * 4 + q) * 1024);
                    SB();
                    MFMA_K(ks == 0, s2[q], v[ks & 1][q], v[ks & 1][q]);
                }
                if (!(ks & 1)) SIGP(13 + ks);
            }
            MFMA_DONE2(s1[3], s2[3]);
            if (g2 == (c2 >> 2)) {
#pragma unroll
                for (int m = 0; m < 4; ++m) { stsf(L_SUM1 + (16 * m + c2) * 4, s1[m][0]); stsf(L_SQ1 + (16 * m + c2) * 4, diag4(s2[m], c2)); }
            }
            SIGP(26);
            PIN(wb4[0]); PIN(wb4[1]); PIN(wb4[2]);
            S4_PRE();
            wait_ge(aC2, 26);
        }
        u32x4 af[2][9];
        if (hf == 0) {
            wait_ge(L_HELP, 101);
            const float gt = ldsf(L_GATE + (16 * mt + c2) * 4);
#pragma unroll
            for (int hd = 0; hd < 2; ++hd) {
                u32x4 kf[4];
#pragma unroll
                for (int i = 0; i < 4; ++i) kf[i] = lds128(MR + (hd * 4 + i) * 1024 + ln * 16);
                f32x4 o0 = mfma(kf[0], phq[hd][0], f32x4{}); o0 = mfma(kf[2], phq[hd][1], o0);
                f32x4 o1 = mfma(kf[1], phq[hd][0], f32x4{}); o1 = mfma(kf[3], phq[hd][1], o1);
                const float den = xg_sum(g2 == 3 ? o1[0] : 0.f) + 1e-6f;
                const float sc = gt * __builtin_amdgcn_rcpf(den);
                u32x4 f = pk8(o0 * sc, o1 * sc);
                if (g2 == 3) { f.z = 0; f.w = 0; }
                sts128(AF_OFF[2] + (1 + hd) * 4096 + mt * 1024 + ln * 16, f);
            }
        }
        SIGP(28);
        {
            wait_ge(aC2, 28);
            {
                u32 ab = (2 * mh4) * 1024 + ln * 16, ab2 = ab + AF_OFF[2];
                PIN(ab); PIN(ab2);
#pragma unroll
                for (int k = 0; k < 9; ++k)
#pragma unroll
                    for (int m = 0; m < 2; ++m) af[m][k] = lds128((k < 6 ? ab + AF_OFF[k / 3] : ab2) + (k % 3) * 4096 + m * 1024);
            }
            SIGP(29);
            u32 lv = peek(aL2);
#pragma unroll
            for (int cb = 1; cb < 8; ++cb) {
                const int p = cb - 1;
                if (cb < 7) { if (__builtin_expect(!__all(lv >= B_LAND[cb < 7 ? cb : 0]), 0)) { wait_ge(aL2, B_LAND[cb < 7 ? cb : 0]); } }
                const u32x4 wo0 = W4_RD(p, 7), wo1 = W4_RD(p, 8);
                f32x4 a4[2];
#pragma unroll
                for (int k = 0; k < 7; ++k) {
                    if (k == 0) asm volatile("v_mfma_f32_16x16x32_bf16 %0, %1, %2, 0" : "=&v"(a4[0]) : "v"(f4[p & 1][0]), "v"(af[0][0]));
                    else MFMA_ACC(a4[0], f4[p & 1][k], af[0][k]);
                    if (cb < 7) f4[cb & 1][k] = W4_RD(cb < 7 ? cb : 0, k);
                    SB();
                    if (k == 0) asm volatile("v_mfma_f32_16x16x32_bf16 %0, %1, %2, 0" : "=&v"(a4[1]) : "v"(f4[p & 1][0]), "v"(af[1][0]));
                    else MFMA_ACC(a4[1], f4[p & 1][k], af[1][k]);
                    if (k == 3) lv = peek(aL2);
                    if (cb <= 3 && k == 5) SIGP(29 + cb);
                    SB();
                }
                MFMA_ACC(a4[0], wo0, af[0][7]); MFMA_ACC(a4[1], wo0, af[1][7]);
                MFMA_ACC(a4[0], wo1, af[0][8]); MFMA_ACC(a4[1], wo1, af[1][8]);
                asm volatile("s_nop 7" : "+v"(a4[0]), "+v"(a4[1]));
                u32x4 u = swap_units(pk(a4[0][0], a4[0][1]), pk(a4[0][2], a4[0][3]), pk(a4[1][0], a4[1][1]), pk(a4[1][2], a4[1][3]));
                stg128(rWS, rsoff + p * 1024, u);
                if (cb == 5) { WAITV(1); PUBLISH_(4, 8, ln < 8 ? WS_MB2 + (rt * 8 + ln) * 512 + 384 + j * 8 : 0xfffffff0u); }
                SB();
            }
        }
        PUBLISH(3, 8, ln < 8 ? WS_MB2 + (rt * 8 + ln) * 512 + 256 + j * 8 : 0xfffffff0u);
        wait_ge(L_HELP, w == 7 ? 102 : 103);
        asm volatile(".Lfk_e2:");
        if (w >= 4) finish_block(w == 7 ? 3 : w);
    }
}
}

extern "C" void kernel_launch(void* const* d_in, const int* in_sizes, int n_in, void* d_out, int out_size, void* d_ws, size_t ws_size,
                              hipStream_t stream) {
    (void)hipFuncSetAttribute((const void*)fused_kernel, hipFuncAttributeMaxDynamicSharedMemorySize, LDS_BYTES);
    fused_kernel<<<256, NWAVE * 64, LDS_BYTES, stream>>>((const u16*)d_in[0], (const u16*)d_in[1], (const u16*)d_in[2], (const u16*)d_in[3],
                                                         (const u16*)d_in[5], (const u16*)d_in[6], (const u16*)d_in[7], (const u16*)d_in[8],
                                                         (const u16*)d_in[9], (u16*)d_out, (unsigned char*)d_ws);
}
```
